```python
import torch

def _softmax(x, axis=-1):
    return torch.softmax(x, dim=axis)
def _gelu(x):
    return torch.nn.functional.gelu(x, approximate="tanh")
def _silu(x):
    return torch.nn.functional.silu(x)
def _sigmoid(x):
    return torch.sigmoid(x)
def _layernorm(x, axis=-1, eps=1e-5):
    m = x.mean(dim=axis, keepdim=True)
    v = x.var(dim=axis, keepdim=True, unbiased=False)
    return (x - m) / torch.sqrt(v + eps)
def _scatter_add(tgt, idx, src, axis=0):
    return tgt.clone().index_add_(axis, idx, src)
def _segment_sum(src, seg, n):
    out = torch.zeros((n,) + src.shape[1:], dtype=src.dtype)
    return out.index_add_(0, seg, src)
def _pad(x, pw):
    flat = tuple(v for pair in reversed(pw) for v in pair)
    return torch.nn.functional.pad(x, flat)

kernel_name = 'parallel_sum__linear_kernel_hedgehog__dense_activation_squared_relu__prompt'

def setup_inputs():

    _w = (lambda a: (lambda t: t.to(torch.bfloat16) if t.is_floating_point() else t)(torch.as_tensor(a)))
    def _A_setup():
        B, S, D, H = 1, 2048, 512, 32
        Dh = D // H
        return {
            "x": _w((lambda *s: torch.randn(*s, dtype=torch.bfloat16) * 0.05)(B, S, D)),
            "wqkv": _w((lambda *s: torch.randn(*s, dtype=torch.bfloat16) * 0.8)(D, 3 * D)),
            "w_phi": _w((lambda *s: torch.randn(*s, dtype=torch.bfloat16) * 0.8)(H, Dh, Dh) * (Dh ** -0.5)),
            "wo": _w((lambda *s: torch.randn(*s, dtype=torch.bfloat16) * 0.2)(D, D)),
        }
    _A_in = _A_setup()

    def _B_setup():
        B, S, D, F = 1, 2048, 512, 1792
        return {
            "x": _w((lambda *s: torch.randn(*s, dtype=torch.bfloat16) * 0.05)(B, S, D)),
            "w1": _w((lambda *s: torch.randn(*s, dtype=torch.bfloat16) * 0.05)(D, F)),
            "b1": _w((lambda *s: torch.randn(*s, dtype=torch.bfloat16) * 0.4)(F)),
            "w2": _w((lambda *s: torch.randn(*s, dtype=torch.bfloat16) * 0.05)(F, D)),
            "b2": _w((lambda *s: torch.randn(*s, dtype=torch.bfloat16) * 3.2)(D)),
        }
    _B_in = _B_setup()

    return {
        "A_x": _A_in["x"],
        "A_wqkv": _A_in["wqkv"],
        "A_w_phi": _A_in["w_phi"],
        "A_wo": _A_in["wo"],
        "B_x": _B_in["x"],
        "B_w1": _B_in["w1"],
        "B_b1": _B_in["b1"],
        "B_w2": _B_in["w2"],
        "B_b2": _B_in["b2"],
    }

def reference(A_x, A_wqkv, A_w_phi, A_wo, B_x, B_w1, B_b1, B_w2, B_b2):
    def _A_ref(x, wqkv, w_phi, wo):
        B, S, D = x.shape
        H = 32; Dh = D // H
        qkv = (lambda x, *d: x.permute(*d))((x @ wqkv).reshape(B, S, 3, H, Dh), 2, 0, 3, 1, 4)
        q, k, v = qkv[0], qkv[1], qkv[2]
        pq = torch.einsum("bhsd,hdf->bhsf", q, w_phi)
        pk = torch.einsum("bhsd,hdf->bhsf", k, w_phi)
        phi_q = torch.concatenate([_softmax(pq, axis=-1), _softmax(-pq, axis=-1)], axis=-1)
        phi_k = torch.concatenate([_softmax(pk, axis=-1), _softmax(-pk, axis=-1)], axis=-1)
        kv = torch.einsum("bhsf,bhsd->bhfd", phi_k, v)
        z  = torch.einsum("bhsf->bhf", phi_k)
        num = torch.einsum("bhqf,bhfd->bhqd", phi_q, kv)
        den = torch.einsum("bhqf,bhf->bhq", phi_q, z)[..., None] + 1e-6
        o = num / den
        return (lambda x, *d: x.permute(*d))(o, 0, 2, 1, 3).reshape(B, S, D) @ wo

    def _B_ref(x, w1, b1, w2, b2):
        h = torch.einsum("bsd,df->bsf", x, w1) + b1
        zero = torch.zeros_like(h)
        r = torch.where(h > zero, h, zero)
        h = r * r
        out = torch.einsum("bsf,fd->bsd", h, w2) + b2
        return out

    x = A_x
    xn = _layernorm(x, axis=-1)
    return x + _A_ref(xn, A_wqkv, A_w_phi, A_wo) + _B_ref(xn, B_w1, B_b1, B_w2, B_b2)
```

```cpp
#include <hip/hip_runtime.h>
#include <stdint.h>

typedef unsigned int u32;
typedef unsigned long long u64;
typedef __bf16 bf16x8 __attribute__((ext_vector_type(8)));
typedef __bf16 bf16x2 __attribute__((ext_vector_type(2)));
typedef short v4i16 __attribute__((ext_vector_type(4)));
typedef float f32x4 __attribute__((ext_vector_type(4)));
typedef float f32x2 __attribute__((ext_vector_type(2)));
typedef u32 u32x4 __attribute__((ext_vector_type(4)));
typedef u32 u32x2 __attribute__((ext_vector_type(2)));

#define AS1 __attribute__((address_space(1)))
#define AS3 __attribute__((address_space(3)))
#define AS4 __attribute__((address_space(4)))
#define DEVI __device__ __forceinline__

constexpr u32 WS_EPOCH = 0;
constexpr u32 WS_AFLAG = 1024;
constexpr u32 WS_HFLAG = 2048;
constexpr u32 WS_OFLAG = 4096;
constexpr u32 WS_KVFLAG = 6144;
constexpr u32 WS_STAT = 8192;
constexpr u32 WS_CS = 40960;
constexpr u32 WS_KVP = 0x50000;
constexpr u32 WS_ZP = 0xD0000;
constexpr u32 WS_AF = 0x100000;
constexpr u32 WS_TIM = 0xB00000;
constexpr u32 WS_USED = 0xC00000;

constexpr u32 SLOT = 49152;
constexpr u32 L_SCR = 131072;
constexpr u32 L_ZR = L_SCR + 24576;
constexpr u32 L_CSB = 157696;
constexpr u32 L_MEAN = 159744;
constexpr u32 L_RSTD = 160256;
constexpr u32 L_WPHI = 160768;
constexpr u32 LDS_BYTES = 162816;

DEVI f32x4 mfma32(u32x4 a, u32x4 b, f32x4 c) {
  return __builtin_amdgcn_mfma_f32_16x16x32_bf16(__builtin_bit_cast(bf16x8, a), __builtin_bit_cast(bf16x8, b), c, 0, 0, 0);
}
DEVI f32x4 mfma16(u32x2 a, u32x2 b, f32x4 c) {
  return __builtin_amdgcn_mfma_f32_16x16x16bf16_1k(__builtin_bit_cast(v4i16, a), __builtin_bit_cast(v4i16, b), c, 0, 0, 0);
}
DEVI u32 pk2(float a, float b) {
  f32x2 v = {a, b};
  return __builtin_bit_cast(u32, __builtin_convertvector(v, bf16x2));
}
DEVI float bflo(u32 v) { return __builtin_bit_cast(float, v << 16); }
DEVI float bfhi(u32 v) { return __builtin_bit_cast(float, v & 0xffff0000u); }
DEVI u32 fbits(float f) { return __builtin_bit_cast(u32, f); }
DEVI float bitsf(u32 u) { return __builtin_bit_cast(float, u); }

DEVI u32x2 lds_tr(AS3 char* L, u32 addr) {
  v4i16 t = __builtin_amdgcn_ds_read_tr16_b64_v4i16((AS3 v4i16*)(L + addr));
  return __builtin_bit_cast(u32x2, t);
}
DEVI u32x4 lds_frag_tr(AS3 char* L, u32 addr, u32 second) {
  u32x2 a = lds_tr(L, addr), b = lds_tr(L, addr + second);
  u32x4 r = {a[0], a[1], b[0], b[1]};
  return r;
}
DEVI u32x4 lds128(AS3 char* L, u32 addr) { return *(AS3 u32x4*)(L + addr); }
DEVI f32x4 lds128f(AS3 char* L, u32 addr) { return *(AS3 f32x4*)(L + addr); }
template <int OFF> DEVI void glds_m0(u32 m0v, const char* sbase, u32 voff) {
  asm volatile("s_mov_b32 m0, %0\n\ts_nop 0\n\tglobal_load_lds_dwordx4 %1, %2 offset:%3" ::"s"(m0v), "v"(voff), "s"(sbase), "n"(OFF) : "memory");
}
template <int OFF> DEVI void glds(const char* sbase, u32 voff) {
  asm volatile("global_load_lds_dwordx4 %0, %1 offset:%2" ::"v"(voff), "s"(sbase), "n"(OFF) : "memory");
}
template <int OFF> DEVI void glds_masked(const char* sbase, u32 voff, u64 mask) {
  asm volatile("s_mov_b64 exec, %3\n\tglobal_load_lds_dwordx4 %0, %1 offset:%2\n\ts_mov_b64 exec, -1" ::"v"(voff), "s"(sbase), "n"(OFF), "s"(mask) : "memory");
}
template <int OFF> DEVI void dsr128(u32x4& d, u32 a) {
  asm volatile("ds_read_b128 %0, %1 offset:%2" : "=v"(d) : "v"(a), "n"(OFF) : "memory");
}
template <int OFF> DEVI void dsrtr(u32x2& d, u32 a) {
  asm volatile("ds_read_b64_tr_b16 %0, %1 offset:%2" : "=v"(d) : "v"(a), "n"(OFF) : "memory");
}
DEVI u32x4 cat(u32x2 a, u32x2 b) { u32x4 r = {a[0], a[1], b[0], b[1]}; return r; }
#define WAIT_V(n) asm volatile("s_waitcnt vmcnt(%0)" ::"n"(n) : "memory")
#define WAIT_L0() asm volatile("s_waitcnt lgkmcnt(0)" ::: "memory")
#define BAR() __builtin_amdgcn_s_barrier()
#define SCHED() __builtin_amdgcn_sched_barrier(0)

DEVI void ld_sc1_x4(u32x4& d, const char* base, u32 off) {
  asm volatile("global_load_dwordx4 %0, %1, %2 sc1" : "=v"(d) : "v"(off), "s"(base) : "memory");
}
DEVI void ld_x4(u32x4& d, const char* base, u32 off) {
  asm volatile("global_load_dwordx4 %0, %1, %2" : "=v"(d) : "v"(off), "s"(base) : "memory");
}
DEVI void ld_x1(u32& d, const char* base, u32 off) {
  asm volatile("global_load_dword %0, %1, %2" : "=v"(d) : "v"(off), "s"(base) : "memory");
}
DEVI void ld_u16(u32& d, const char* base, u32 off) {
  asm volatile("global_load_ushort %0, %1, %2" : "=v"(d) : "v"(off), "s"(base) : "memory");
}
DEVI u32 ld_sc1_x1_wait(const char* base, u32 off) {
  u32 d;
  asm volatile("global_load_dword %0, %1, %2 sc1\n\ts_waitcnt vmcnt(0)" : "=v"(d) : "v"(off), "s"(base) : "memory");
  return d;
}
DEVI void st_sc1_x4(char* base, u32 off, u32x4 v) {
  asm volatile("global_store_dwordx4 %0, %1, %2 sc1\n\ts_nop 1" ::"v"(off), "v"(v), "s"(base) : "memory");
}
DEVI void st_sc1_x2(char* base, u32 off, u32x2 v) {
  asm volatile("global_store_dwordx2 %0, %1, %2 sc1" ::"v"(off), "v"(v), "s"(base) : "memory");
}
DEVI void st_sc1_x1(char* base, u32 off, u32 v) {
  asm volatile("global_store_dword %0, %1, %2 sc1" ::"v"(off), "v"(v), "s"(base) : "memory");
}
constexpr int SPIN_MAX = 1 << 17;
DEVI void poll_flags(const char* base, u32 off0, int n, int l, u32 tag) {
  u32 off = off0 + (u32)(l < n ? l : 0) * 4u;
#pragma unroll 1
  for (int it = 0; it < SPIN_MAX; ++it) {
    u32 v = ld_sc1_x1_wait(base, off);
    if (__builtin_amdgcn_ballot_w64(v != tag) == 0ull) break;
    __builtin_amdgcn_s_sleep(1);
  }
  __builtin_amdgcn_fence(__ATOMIC_ACQUIRE, "wavefront");
}

#define STAMP(i) do {} while (0)

DEVI void red4_max2(float& a, float& b) {
  auto s = __builtin_amdgcn_permlane32_swap(fbits(a), fbits(b), false, false);
  float t = fmaxf(bitsf(s[0]), bitsf(s[1]));
  auto u = __builtin_amdgcn_permlane16_swap(fbits(t), fbits(t), false, false);
  float m = fmaxf(bitsf(u[0]), bitsf(u[1]));
  auto z = __builtin_amdgcn_permlane32_swap(fbits(m), fbits(m), false, false);
  a = bitsf(z[0]); b = bitsf(z[1]);
}
DEVI void red4_sum2_rcp(float& a, float& b) {
  auto s = __builtin_amdgcn_permlane32_swap(fbits(a), fbits(b), false, false);
  float t = bitsf(s[0]) + bitsf(s[1]);
  auto u = __builtin_amdgcn_permlane16_swap(fbits(t), fbits(t), false, false);
  float m = __builtin_amdgcn_rcpf(bitsf(u[0]) + bitsf(u[1]));
  auto z = __builtin_amdgcn_permlane32_swap(fbits(m), fbits(m), false, false);
  a = bitsf(z[0]); b = bitsf(z[1]);
}
DEVI u32x4 dual_softmax(f32x4 p) {
  const float L2E = 1.4426950408889634f;
  float mx = fmaxf(fmaxf(p[0], p[1]), fmaxf(p[2], p[3]));
  float nm = fmaxf(fmaxf(-p[0], -p[1]), fmaxf(-p[2], -p[3]));
  red4_max2(mx, nm);
  float a = -mx * L2E, c = -nm * L2E;
  float e0 = __builtin_amdgcn_exp2f(fmaf(p[0], L2E, a)), e1 = __builtin_amdgcn_exp2f(fmaf(p[1], L2E, a));
  float e2 = __builtin_amdgcn_exp2f(fmaf(p[2], L2E, a)), e3 = __builtin_amdgcn_exp2f(fmaf(p[3], L2E, a));
  float n0 = __builtin_amdgcn_exp2f(fmaf(p[0], -L2E, c)), n1 = __builtin_amdgcn_exp2f(fmaf(p[1], -L2E, c));
  float n2 = __builtin_amdgcn_exp2f(fmaf(p[2], -L2E, c)), n3 = __builtin_amdgcn_exp2f(fmaf(p[3], -L2E, c));
  float sp = (e0 + e1) + (e2 + e3), sn = (n0 + n1) + (n2 + n3);
  red4_sum2_rcp(sp, sn);
  u32x4 r = {pk2(e0 * sp, e1 * sp), pk2(e2 * sp, e3 * sp), pk2(n0 * sn, n1 * sn), pk2(n2 * sn, n3 * sn)};
  return r;
}

template <int V> struct IC { static constexpr int value = V; };
template <int N> DEVI void wait4(u32x4& a, u32x4& b, u32x4& c, u32x4& d) {
  asm volatile("s_waitcnt vmcnt(%4)" : "+v"(a), "+v"(b), "+v"(c), "+v"(d) : "n"(N) : "memory");
}
template <int N> DEVI void wait1(u32x4& a) { asm volatile("s_waitcnt vmcnt(%1)" : "+v"(a) : "n"(N) : "memory"); }

template <int N> DEVI void waitset(u32x4& a, u32x4& b, u32x2 (&lo)[8], u32x2 (&hi)[8]);
template <> DEVI void waitset<8>(u32x4& a, u32x4& b, u32x2 (&lo)[8], u32x2 (&hi)[8]) {
  asm volatile("s_waitcnt lgkmcnt(0)"
               : "+v"(a), "+v"(b), "+v"(lo[0]), "+v"(lo[1]), "+v"(lo[2]), "+v"(lo[3]), "+v"(lo[4]), "+v"(lo[5]), "+v"(lo[6]), "+v"(lo[7]),
                 "+v"(hi[0]), "+v"(hi[1]), "+v"(hi[2]), "+v"(hi[3]), "+v"(hi[4]), "+v"(hi[5]), "+v"(hi[6]), "+v"(hi[7])
               :: "memory");
}
template <> DEVI void waitset<6>(u32x4& a, u32x4& b, u32x2 (&lo)[8], u32x2 (&hi)[8]) {
  asm volatile("s_waitcnt lgkmcnt(0)"
               : "+v"(a), "+v"(b), "+v"(lo[0]), "+v"(lo[1]), "+v"(lo[2]), "+v"(lo[3]), "+v"(lo[4]), "+v"(lo[5]),
                 "+v"(hi[0]), "+v"(hi[1]), "+v"(hi[2]), "+v"(hi[3]), "+v"(hi[4]), "+v"(hi[5])
               :: "memory");
}
DEVI void waitb(u32x2 (&lo)[4], u32x2 (&hi)[4]) {
  asm volatile("s_waitcnt lgkmcnt(0)"
               : "+v"(lo[0]), "+v"(lo[1]), "+v"(lo[2]), "+v"(lo[3]), "+v"(hi[0]), "+v"(hi[1]), "+v"(hi[2]), "+v"(hi[3]) :: "memory");
}
DEVI float wave_sum(float v) {
#define DPP_ADD(ctrl, rmask) v += bitsf((u32)__builtin_amdgcn_update_dpp(0, (int)fbits(v), ctrl, rmask, 0xf, false))
  DPP_ADD(0x111, 0xf); DPP_ADD(0x112, 0xf); DPP_ADD(0x114, 0xf); DPP_ADD(0x118, 0xf);
  DPP_ADD(0x142, 0xa); DPP_ADD(0x143, 0xc);
#undef DPP_ADD
  return bitsf((u32)__builtin_amdgcn_readlane((int)fbits(v), 63));
}

extern "C" __global__ void __launch_bounds__(512, 2)
fused_kernel(const char* __restrict__ x, const char* __restrict__ wqkv, const char* __restrict__ wphi,
             const char* __restrict__ wo, const char* __restrict__ w1, const char* __restrict__ b1,
             const char* __restrict__ w2, const char* __restrict__ b2, char* __restrict__ out, char* __restrict__ ws) {
  extern __shared__ __attribute__((aligned(16))) char smem[];
  AS3 char* L = (AS3 char*)smem;
  asm volatile("" ::"s"(x), "s"(wqkv), "s"(wphi), "s"(wo), "s"(w1), "s"(b1), "s"(w2), "s"(b2), "s"(out), "s"(ws));
  const int b = blockIdx.x;
  const int l = threadIdx.x & 63;
  const int w = __builtin_amdgcn_readfirstlane(threadIdx.x >> 6);
  const int xcd = b & 7, bi = b >> 3;
  const int p = (xcd >> 2) * 8 + (bi >> 2), c = (xcd & 3) * 4 + (bi & 3), g = c >> 1;
  const bool typeA = (c & 1) == 0;
  const int rg = w & 3, cg = w >> 2;
  const int gq = l >> 4, r16 = l & 15, q4 = (l >> 2) & 3, pp = l & 3;
  const int rot = (p + c) & 7;
  const int rb = 4 * xcd + (bi >> 3), cb = bi & 7;
  STAMP(0);

  const u32 lb = (u32)(size_t)L;
  const u32 xrow_off = (u32)(128 * p + 8 * c + w) * 1024u + (u32)l * 16u;
  u32 gb1[2];
  const u32 gb1_off = (u32)(224 * g + (w < 6 ? 32 * w + ((g & 1) ? 32 : 0) : (w == 6 && !(g & 1) ? 192 : 0)) + (l >> 2)) * 2u;
  u32 tag;

  const char* xsrc = x + (size_t)p * 131072u;
  const u32 wstride = typeA ? 3072u : 3584u;
  const char* wsrc = (typeA ? (wqkv + g * 128) : (w1 + 448 * g)) - 4096;
  const u32 vx0 = (u32)(16 * w + (l >> 3)) * 1024u + (u32)(((l & 7) ^ (l >> 4)) * 16);
  const u32 vx1 = (u32)(16 * w + 7 + (l >> 3)) * 1024u + (u32)(((l & 7) ^ (4 + (l >> 4))) * 16);
  const u32 wsp = (u32)(((l & 7) >> 1) ^ (((l >> 4) & 1) | ((w & 1) << 1)));
  const u32 vw = (u32)(8 * w + (l >> 3)) * wstride + (wsp * 2u + (u32)(l & 1)) * 16u + 4096u;
  u32 vws[4];
  {
    const u32 odd = (u32)(g & 1) * 64u;
    vws[0] = typeA ? vw : vw + odd;
    vws[1] = typeA ? vw : vw + odd + 128u - 1024u;
    vws[2] = typeA ? vw : vw + odd + 256u - 2048u;
    vws[3] = vw + (odd ? 0u : 384u) - 3072u;
  }
  const u64 hmask = __builtin_amdgcn_ballot_w64(wsp < 2u);
  const u32 m0x = lb + (u32)w * 2048u, m0w = lb + 16384u + (u32)w * 4096u;

  const char* dsrc2 = w2 + (size_t)(7 * w) * 32768u + cb * 128;
  const char* dsrco = wo + (size_t)(2 * w) * 32768u + cb * 128;
  const u32 vdb = (u32)(l >> 3) * 1024u + (u32)(((((l & 7) >> 1) ^ ((l >> 4) & 3)) * 2 + (l & 1)) * 16);
  auto dslot = [&](int j) -> u32 { return (u32)((j + 3) & 3) * 32768u + (u32)w * 4096u; };
  auto issueB = [&](int j) {
    const char* s = (j < 7) ? (dsrc2 + j * 32768) : (dsrco + (j - 7) * 32768);
    glds_m0<0>(lb + dslot(j), s, vdb);
    glds<1024>(s, vdb + 7168u);
    glds<2048>(s, vdb + 2u * 7168u);
    glds<3072>(s, vdb + 3u * 7168u);
  };

  u32 xa0 = lb + (u32)(32 * rg + r16) * 128u + (u32)((gq ^ ((r16 >> 1) & 7)) * 16);
  u32 xa1 = xa0 ^ 64u;
  const u32 vbase = lb + 16384u + (u32)gq * 4096u + (u32)q4 * 128u + (u32)pp * 8u + (u32)(((q4 >> 1) & 1) | ((gq & 1) << 1)) * 32u;
  u32 R[8];
#pragma unroll
  for (int t = 0; t < 8; ++t) {
    const u32 ra = (vbase ^ (u32)((2 * cg + (t & 1)) * 32)) + (u32)(t >> 1) * 1024u;
    const u32 rbb = (vbase ^ (u32)((t & 3) * 32)) + (u32)(t >> 2) * 1024u + (u32)cg * 2048u;
    R[t] = typeA ? ra : rbb;
  }

  f32x4 acc[8][2];
#pragma unroll
  for (int t = 0; t < 8; ++t) { acc[t][0] = f32x4{0, 0, 0, 0}; acc[t][1] = f32x4{0, 0, 0, 0}; }
  u32x4 gat[8];
  const u32x4 ones = {0x3F803F80u, 0x3F803F80u, 0x3F803F80u, 0x3F803F80u};
  const u32 gcs = WS_CS + (u32)(c * 256 + 32 * w + (l >> 2)) * 64u + (u32)(l & 3) * 16u;
  const u32 gst = WS_STAT + (u32)(128 * p + 16 * w + r16) * 16u;

  auto g1 = [&](auto TAc, auto N8c) {
    constexpr bool TA = decltype(TAc)::value != 0, N8 = decltype(N8c)::value != 0;
    constexpr int NG = TA ? 5 : 6;
    u32x4 fx[2][2];
    u32x2 flo[2][8], fhi[2][8];
    u32x4 xrow;
    u32 epoch;
    asm volatile("s_load_dword %0, %1, 0x0" : "=s"(epoch) : "s"(ws) : "memory");
    ld_x4(xrow, x, xrow_off);
    if (TA) { gb1[0] = 0; gb1[1] = 0; }
    else { ld_u16(gb1[0], b1, gb1_off); ld_u16(gb1[1], b1, gb1_off + 32u); }
    auto issue = [&](int s, u32 sb) {
      const int ks = (s + rot) & 7;
      const char* xs = xsrc + ks * 128;
      const char* wsb = wsrc + (size_t)((u32)ks * 64u * wstride);
      glds_m0<0>(m0x + sb, xs, vx0);
      glds<1024>(xs, vx1);
      glds_m0<0>(m0w + sb, wsb, vws[0]);
      glds<1024>(wsb, vws[1]);
      glds<2048>(wsb, vws[2]);
      if (!TA) glds_masked<3072>(wsb, vws[3], hmask);
    };
    issue(0, 0); issue(1, SLOT); issue(2, 2 * SLOT);
    if (TA && w < 2) glds_m0<0>(lb + L_WPHI + (u32)w * 1024u, wphi + g * 2048 + w * 1024, (u32)l * 16u);

#define MF(S, t, rt) acc[t][rt] = mfma32(cat(flo[S][t], fhi[S][t]), fx[S][rt], acc[t][rt])
#define MFS(S, t, rt) acc[t][rt] = mfma32(fx[S][rt], cat(flo[S][t], fhi[S][t]), acc[t][rt])
#define RW(N, t) do { if (RD) { dsrtr<(N) * 16384>(flo[N][t], R[t]); dsrtr<(N) * 16384 + 512>(fhi[N][t], R[t]); } } while (0)
#define RX(N) do { if (RD) { dsr128<0>(fx[N][0], (N) ? xa1 : xa0); dsr128<2048>(fx[N][1], (N) ? xa1 : xa0); } } while (0)
    auto waitS = [&](auto Sc) {
      constexpr int S = decltype(Sc)::value;
      if constexpr (N8) waitset<8>(fx[S][0], fx[S][1], flo[S], fhi[S]);
      else waitset<6>(fx[S][0], fx[S][1], flo[S], fhi[S]);
    };
    auto phase = [&](auto Sc, auto RDc, auto GLc, const char* xs, const char* wsb, u32 sb) {
      constexpr int S = decltype(Sc)::value, N = 1 - S;
      constexpr bool RD = decltype(RDc)::value != 0, GL = decltype(GLc)::value != 0;
      waitS(Sc);
      if (S == 1 && RD) BAR();
      SCHED();
      if constexpr (N8) {
        MF(S, 6, 0); RW(N, 6); SCHED();
        MF(S, 6, 1); RW(N, 7); SCHED();
        MF(S, 7, 0); SCHED();
        MF(S, 7, 1); SCHED();
      }
      if constexpr (TA) {
        MFS(S, 4, 0); RX(N); SCHED();
        MFS(S, 4, 1); RW(N, 4); SCHED();
        MFS(S, 5, 0); RW(N, 5); SCHED();
        MFS(S, 5, 1); RW(N, 0); SCHED();
      } else {
        MF(S, 4, 0); RX(N); SCHED();
        MF(S, 4, 1); RW(N, 4); SCHED();
        MF(S, 5, 0); RW(N, 5); SCHED();
        MF(S, 5, 1); RW(N, 0); SCHED();
      }
      MF(S, 0, 0); RW(N, 1); SCHED();
      MF(S, 0, 1); RW(N, 2); SCHED();
      MF(S, 1, 0); RW(N, 3); SCHED();
      MF(S, 1, 1); if (GL) { glds_m0<0>(m0x + sb, xs, vx0); glds<1024>(xs, vx1); } SCHED();
      MF(S, 2, 0); if (GL) glds_m0<0>(m0w + sb, wsb, vws[0]); SCHED();
      MF(S, 2, 1); if (GL) glds<1024>(wsb, vws[1]); SCHED();
      MF(S, 3, 0); if (GL) glds<2048>(wsb, vws[2]); SCHED();
      MF(S, 3, 1); if (GL && !TA) glds_masked<3072>(wsb, vws[3], hmask); SCHED();
    };
    auto advance = [&](u32 d) {
#pragma unroll
      for (int t = 0; t < (N8 ? 8 : 6); ++t) R[t] += d;
      xa0 += d; xa1 += d;
    };

    STAMP(1);
    if (TA) asm volatile("s_waitcnt vmcnt(%1)" : "+v"(xrow) : "n"(2 * NG) : "memory");
    else asm volatile("s_waitcnt vmcnt(%3)" : "+v"(xrow), "+v"(gb1[0]), "+v"(gb1[1]) : "n"(2 * NG) : "memory");
    BAR();
    {
      constexpr bool RD = true;
      RX(0); RW(0, 4); RW(0, 5); RW(0, 0); RW(0, 1); RW(0, 2); RW(0, 3);
      if constexpr (N8) { RW(0, 6); RW(0, 7); }
    }
    waitS(IC<0>{});
    asm volatile("" : "+s"(epoch) :: "memory");
    tag = epoch + 1u;
    phase(IC<0>{}, IC<1>{}, IC<0>{}, nullptr, nullptr, 0u);
    {
      float v[8];
#pragma unroll
      for (int i = 0; i < 4; ++i) { v[2 * i] = bflo(xrow[i]); v[2 * i + 1] = bfhi(xrow[i]); }
      const float mean = wave_sum(((v[0] + v[1]) + (v[2] + v[3])) + ((v[4] + v[5]) + (v[6] + v[7]))) * (1.0f / 512.0f);
      float qq = 0.f;
#pragma unroll
      for (int i = 0; i < 8; ++i) { float d = v[i] - mean; qq = fmaf(d, d, qq); }
      const float rstd = 1.0f / sqrtf(wave_sum(qq) * (1.0f / 512.0f) + 1e-5f);
      if (l == 0) {
        u32x4 gr = {fbits(mean), fbits(rstd), tag, tag};
        st_sc1_x4(ws, WS_STAT + (u32)(128 * p + 8 * c + w) * 16u, gr);
        if (w == 7) st_sc1_x1(ws, WS_AFLAG + (u32)b * 4u, tag);
      }
      if (p < 8 && w < (TA ? 6 : 7)) {
        u32x2 c0l[2], c0h[2], c1l[2], c1h[2];
        const u32 a0 = (vbase ^ (u32)((2 * (w & 1)) * 32)) + (u32)(w >> 1) * 1024u;
        const u32 a1 = a0 ^ 32u;
        dsrtr<0>(c0l[0], a0); dsrtr<512>(c0h[0], a0); dsrtr<16384>(c0l[1], a0); dsrtr<16384 + 512>(c0h[1], a0);
        dsrtr<0>(c1l[0], a1); dsrtr<512>(c1h[0], a1); dsrtr<16384>(c1l[1], a1); dsrtr<16384 + 512>(c1h[1], a1);
        asm volatile("s_waitcnt lgkmcnt(0)" : "+v"(c0l[0]), "+v"(c0h[0]), "+v"(c0l[1]), "+v"(c0h[1]),
                     "+v"(c1l[0]), "+v"(c1h[0]), "+v"(c1l[1]), "+v"(c1h[1]) :: "memory");
        f32x4 c0 = {0, 0, 0, 0}, c1 = {0, 0, 0, 0};
#pragma unroll
        for (int ks = 0; ks < 2; ++ks) {
          c0 = mfma32(ones, cat(c0l[ks], c0h[ks]), c0);
          c1 = mfma32(ones, cat(c1l[ks], c1h[ks]), c1);
        }
        if (l < 32) {
          u32x2 gr = {fbits(l < 16 ? c0[0] : c1[0]), tag};
          st_sc1_x2(ws, WS_CS + (u32)((c * 256 + 32 * w + l) * 8 + rot) * 8u, gr);
        }
      }
    }
    u32 sb = 0;
#pragma unroll 1
    for (int s = 0; s < 5; ++s) {
      const u32 nsb = sb == 2 * SLOT ? 0u : sb + SLOT;
      advance(nsb - sb);
      const int ks = (s + 3 + rot) & 7;
      WAIT_V(NG);
      phase(IC<1>{}, IC<1>{}, IC<1>{}, xsrc + ks * 128, wsrc + (size_t)((u32)ks * 64u * wstride), sb);
      phase(IC<0>{}, IC<1>{}, IC<0>{}, nullptr, nullptr, 0u);
      sb = nsb;
    }
    advance(0u - 2u * SLOT);
    WAIT_V(NG);
    phase(IC<1>{}, IC<1>{}, IC<0>{}, nullptr, nullptr, 0u);
    ld_sc1_x4(gat[0], ws, gcs);
    ld_sc1_x4(gat[1], ws, gcs + 1024u);
    ld_sc1_x4(gat[2], ws, gst);
    issueB(0);
    phase(IC<0>{}, IC<1>{}, IC<0>{}, nullptr, nullptr, 0u);
    advance(SLOT);
    WAIT_V(7);
    phase(IC<1>{}, IC<1>{}, IC<0>{}, nullptr, nullptr, 0u);
    issueB(1);
    phase(IC<0>{}, IC<1>{}, IC<0>{}, nullptr, nullptr, 0u);
    phase(IC<1>{}, IC<0>{}, IC<0>{}, nullptr, nullptr, 0u);
#undef MF
#undef MFS
#undef RW
#undef RX
    STAMP(2);
    asm volatile("s_waitcnt vmcnt(8)" : "+v"(gat[0]), "+v"(gat[1]), "+v"(gat[2]) :: "memory");
  };
  if (typeA) g1(IC<1>{}, IC<0>{});
  else if (cg == 0) g1(IC<0>{}, IC<1>{});
  else g1(IC<0>{}, IC<0>{});
  {
    const bool used = w < (typeA ? 6 : 7);
  #pragma unroll 1
  for (int it = 0; it < SPIN_MAX; ++it) {
      const bool bad = (used && ((gat[0][1] != tag) | (gat[0][3] != tag) | (gat[1][1] != tag) | (gat[1][3] != tag))) | (gat[2][2] != tag);
      if (__builtin_amdgcn_ballot_w64(bad) == 0ull) break;
      ld_sc1_x4(gat[0], ws, gcs);
      ld_sc1_x4(gat[1], ws, gcs + 1024u);
      ld_sc1_x4(gat[2], ws, gst);
      asm volatile("s_waitcnt vmcnt(0)" : "+v"(gat[0]), "+v"(gat[1]), "+v"(gat[2]) :: "memory");
    }
#pragma unroll
    for (int i = 0; i < 2; ++i) {
      float sm = bitsf(gat[i][0]) + bitsf(gat[i][2]);
      sm += bitsf((u32)__builtin_amdgcn_update_dpp(0, (int)fbits(sm), 0xB1, 0xf, 0xf, false));
      sm += bitsf((u32)__builtin_amdgcn_update_dpp(0, (int)fbits(sm), 0x4E, 0xf, 0xf, false));
      if ((l & 3) == 0) {
        f32x2 o = {sm, bflo(gb1[i])};
        *(AS3 f32x2*)(L + L_CSB + (u32)(32 * w + 16 * i + (l >> 2)) * 8u) = o;
      }
    }
    if (l < 16) {
      *(AS3 float*)(L + L_MEAN + (u32)(16 * w + l) * 4u) = bitsf(gat[2][0]);
      *(AS3 float*)(L + L_RSTD + (u32)(16 * w + l) * 4u) = bitsf(gat[2][1]);
    }
  }
  WAIT_L0();
  BAR();
  issueB(2); issueB(3);
  STAMP(3);

  const float mean0 = *(AS3 float*)(L + L_MEAN + (u32)(32 * rg + r16) * 4u);
  const float mean1 = *(AS3 float*)(L + L_MEAN + (u32)(32 * rg + 16 + r16) * 4u);
  const float rstd0 = *(AS3 float*)(L + L_RSTD + (u32)(32 * rg + r16) * 4u);
  const float rstd1 = *(AS3 float*)(L + L_RSTD + (u32)(32 * rg + 16 + r16) * 4u);

  const u32 vb2 = (u32)(4 * gq + q4) * 128u + (u32)pp * 8u + (u32)((2 * gq + (q4 >> 1)) & 3) * 32u;
  u32x2 blo[4][4], bhi[4][4];
  u32 VB[4];
#pragma unroll
  for (int ct = 0; ct < 4; ++ct) VB[ct] = lb + (vb2 ^ (u32)(ct * 32)) + (u32)w * 4096u;
  auto rdB = [&](auto jc) {
    constexpr int j = decltype(jc)::value, blk = (j + 3) & 3;
#pragma unroll
    for (int ct = 0; ct < 4; ++ct) {
      const u32 a = VB[ct] + (blk >= 2 ? 65536u : 0u);
      dsrtr<(blk & 1) * 32768>(blo[j & 3][ct], a);
      dsrtr<(blk & 1) * 32768 + 2048>(bhi[j & 3][ct], a);
    }
  };
  auto loadBreg = [&]() {
    rdB(IC<0>{}); rdB(IC<1>{}); waitb(blo[0], bhi[0]); waitb(blo[1], bhi[1]);
    rdB(IC<2>{}); rdB(IC<3>{}); waitb(blo[2], bhi[2]); waitb(blo[3], bhi[3]);
    issueB(4); issueB(5); issueB(6); issueB(7);
  };

  if (!typeA) {
    auto hpair = [&](auto Pc) {
      constexpr int P = decltype(Pc)::value;
      const int hl = cg * 4 + P;
      u32x4 o0, o1;
#pragma unroll
      for (int e = 0; e < 2; ++e) {
        const int t = 2 * P + e;
        const u32 ca = L_CSB + (u32)(hl * 32 + e * 16 + 4 * gq) * 8u;
        const f32x4 cb0 = lds128f(L, ca), cb1 = lds128f(L, ca + 16u);
        const float cs[4] = {cb0[0], cb0[2], cb1[0], cb1[2]}, bb[4] = {cb0[1], cb0[3], cb1[1], cb1[3]};
        float h0[4], h1[4];
#pragma unroll
        for (int r = 0; r < 4; ++r) {
          float y0 = fmaf(rstd0, fmaf(-mean0, cs[r], acc[t][0][r]), bb[r]);
          float y1 = fmaf(rstd1, fmaf(-mean1, cs[r], acc[t][1][r]), bb[r]);
          y0 = fmaxf(y0, 0.f); y1 = fmaxf(y1, 0.f);
          h0[r] = y0 * y0; h1[r] = y1 * y1;
        }
        o0[2 * e] = pk2(h0[0], h0[1]); o0[2 * e + 1] = pk2(h0[2], h0[3]);
        o1[2 * e] = pk2(h1[0], h1[1]); o1[2 * e + 1] = pk2(h1[2], h1[3]);
      }
      const u32 step = (u32)(7 * g + ((g & 1) ? (hl < 6 ? hl + 1 : 0) : hl));
      const u32 off = WS_AF + ((u32)(8 * p + 2 * rg) * 72u + step) * 1024u + (u32)l * 16u;
      st_sc1_x4(ws, off, o0);
      st_sc1_x4(ws, off + 72u * 1024u, o1);
    };
    hpair(IC<0>{}); hpair(IC<1>{}); hpair(IC<2>{});
    if (cg == 0) hpair(IC<3>{});
    WAIT_V(0);
    BAR();
    if (threadIdx.x == 0) st_sc1_x1(ws, WS_HFLAG + (u32)p * 128u + (u32)g * 4u, tag);
    STAMP(4);
    loadBreg();
  } else {
    const u32x2 ident = {(4 * gq + 0 == r16 ? 0x3F80u : 0u) | (4 * gq + 1 == r16 ? 0x3F800000u : 0u),
                         (4 * gq + 2 == r16 ? 0x3F80u : 0u) | (4 * gq + 3 == r16 ? 0x3F800000u : 0u)};
    const f32x4 zero4 = {0, 0, 0, 0};
    const f32x4 vm0 = lds128f(L, L_MEAN + (u32)(32 * rg + 4 * gq) * 4u), vm1 = lds128f(L, L_MEAN + (u32)(32 * rg + 16 + 4 * gq) * 4u);
    const f32x4 vr0 = lds128f(L, L_RSTD + (u32)(32 * rg + 4 * gq) * 4u), vr1 = lds128f(L, L_RSTD + (u32)(32 * rg + 16 + 4 * gq) * 4u);
    u32x2 wp[2];
    auto logits = [&](int line, int pi, int rt) -> f32x4 {
      const int t = line * 2 + pi;
      const u32 ca = L_CSB + (u32)(line * 64 + (2 * cg + pi) * 16 + 4 * gq) * 8u;
      const f32x4 cb0 = lds128f(L, ca), cb1 = lds128f(L, ca + 16u);
      const float mn = rt ? mean1 : mean0, rs = rt ? rstd1 : rstd0;
      const float y0 = rs * fmaf(-mn, cb0[0], acc[t][rt][0]), y1 = rs * fmaf(-mn, cb0[2], acc[t][rt][1]);
      const float y2 = rs * fmaf(-mn, cb1[0], acc[t][rt][2]), y3 = rs * fmaf(-mn, cb1[2], acc[t][rt][3]);
      const u32x2 kb = {pk2(y0, y1), pk2(y2, y3)};
      return mfma16(wp[pi], kb, zero4);
    };
#pragma unroll
    for (int pi = 0; pi < 2; ++pi)
      wp[pi] = lds_tr(L, L_WPHI + (u32)((2 * cg + pi) * 512 + (4 * gq + q4) * 32 + pp * 8));
    f32x4 kvt[4], zt[4];
#pragma unroll
    for (int pi = 0; pi < 2; ++pi) {
      u32x4 tp, tn, vv;
#pragma unroll
      for (int rt = 0; rt < 2; ++rt) {
        const u32x4 ph = dual_softmax(logits(1, pi, rt));
        const f32x4 a = mfma16(u32x2{ph[0], ph[1]}, ident, zero4);
        const f32x4 n = mfma16(u32x2{ph[2], ph[3]}, ident, zero4);
        tp[2 * rt] = pk2(a[0], a[1]); tp[2 * rt + 1] = pk2(a[2], a[3]);
        tn[2 * rt] = pk2(n[0], n[1]); tn[2 * rt + 1] = pk2(n[2], n[3]);
        const float cs = *(AS3 float*)(L + L_CSB + (u32)(128 + (2 * cg + pi) * 16 + r16) * 8u);
        const f32x4 vm = rt ? vm1 : vm0, vr = rt ? vr1 : vr0;
        const f32x4 av = acc[4 + pi][rt];
        vv[2 * rt] = pk2(vr[0] * fmaf(-vm[0], cs, av[0]), vr[1] * fmaf(-vm[1], cs, av[1]));
        vv[2 * rt + 1] = pk2(vr[2] * fmaf(-vm[2], cs, av[2]), vr[3] * fmaf(-vm[3], cs, av[3]));
      }
      kvt[2 * pi] = mfma32(tp, vv, zero4); kvt[2 * pi + 1] = mfma32(tn, vv, zero4);
      zt[2 * pi] = mfma32(tp, ones, zero4); zt[2 * pi + 1] = mfma32(tn, ones, zero4);
    }
    {
      const int sl = rg > 0 ? rg - 1 : 0;
#pragma unroll
      for (int pi = 0; pi < 2; ++pi) {
        if (rg != pi) {
#pragma unroll
          for (int sg = 0; sg < 2; ++sg)
            *(AS3 f32x4*)(L + L_SCR + (u32)((((cg * 2 + pi) * 2 + sg) * 3 + sl) * 1024 + l * 16)) = kvt[2 * pi + sg];
        }
        if (r16 == 0) {
#pragma unroll
          for (int sg = 0; sg < 2; ++sg)
            *(AS3 f32x4*)(L + L_ZR + (u32)(((((cg * 2 + pi) * 2 + sg) * 4 + rg) * 16 + 4 * gq) * 4)) = zt[2 * pi + sg];
        }
      }
    }
    WAIT_L0();
    BAR();
    if (rg < 2) {
      f32x4 sp = rg == 0 ? kvt[0] : kvt[2], sn = rg == 0 ? kvt[1] : kvt[3];
#pragma unroll
      for (int sl = 0; sl < 3; ++sl) {
        sp += lds128f(L, L_SCR + (u32)((((cg * 2 + rg) * 2 + 0) * 3 + sl) * 1024 + l * 16));
        sn += lds128f(L, L_SCR + (u32)((((cg * 2 + rg) * 2 + 1) * 3 + sl) * 1024 + l * 16));
      }
      u32x4 o = {pk2(sp[0], sp[1]), pk2(sp[2], sp[3]), pk2(sn[0], sn[1]), pk2(sn[2], sn[3])};
      st_sc1_x4(ws, WS_KVP + (u32)(((g * 16 + p) * 4 + 2 * cg + rg) * 1024 + l * 16), o);
    } else if (l < 8) {
      const int pi = rg - 2, sg = l >> 2, qq = l & 3;
      f32x4 s = {0, 0, 0, 0};
#pragma unroll
      for (int r = 0; r < 4; ++r) s += lds128f(L, L_ZR + (u32)(((((cg * 2 + pi) * 2 + sg) * 4 + r) * 16 + 4 * qq) * 4));
      st_sc1_x4(ws, WS_ZP + (u32)((((g * 4 + 2 * cg + pi) * 16 + p) * 32 + l * 4) * 4), __builtin_bit_cast(u32x4, s));
    }
    WAIT_V(0);
    BAR();
    if (threadIdx.x == 0) st_sc1_x1(ws, WS_KVFLAG + (u32)g * 128u + (u32)p * 4u, tag);
    STAMP(4);
    loadBreg();
    u32x4 phq[2][2];
#pragma unroll
    for (int pi = 0; pi < 2; ++pi)
#pragma unroll
      for (int rt = 0; rt < 2; ++rt) phq[pi][rt] = dual_softmax(logits(0, pi, rt));
    STAMP(5);
    if (w == 0) poll_flags(ws, WS_KVFLAG + (u32)g * 128u, 16, l, tag);
    BAR();
    STAMP(6);
    {
      const int hd = w & 3, hf = w >> 2;
#pragma unroll
      for (int i = 0; i < 8; ++i) ld_sc1_x4(gat[i], ws, WS_KVP + (u32)(((g * 16 + 8 * hf + i) * 4 + hd) * 1024 + l * 16));
      u32x4 zg;
      ld_sc1_x4(zg, ws, WS_ZP + (u32)(((g * 4 + hd) * 16 + 8 * hf) * 128 + l * 16));
      asm volatile("s_waitcnt vmcnt(0)" : "+v"(gat[0]), "+v"(gat[1]), "+v"(gat[2]), "+v"(gat[3]), "+v"(gat[4]), "+v"(gat[5]),
                   "+v"(gat[6]), "+v"(gat[7]), "+v"(zg) :: "memory");
      f32x4 s0 = {0, 0, 0, 0}, s1 = {0, 0, 0, 0};
#pragma unroll
      for (int i = 0; i < 8; ++i) {
        s0[0] += bflo(gat[i][0]); s0[1] += bfhi(gat[i][0]); s0[2] += bflo(gat[i][1]); s0[3] += bfhi(gat[i][1]);
        s1[0] += bflo(gat[i][2]); s1[1] += bfhi(gat[i][2]); s1[2] += bflo(gat[i][3]); s1[3] += bfhi(gat[i][3]);
      }
      *(AS3 f32x4*)(L + L_SCR + (u32)((hd * 2 + hf) * 2048 + l * 32)) = s0;
      *(AS3 f32x4*)(L + L_SCR + (u32)((hd * 2 + hf) * 2048 + l * 32 + 16)) = s1;
      f32x4 zs = __builtin_bit_cast(f32x4, zg);
#pragma unroll
      for (int r = 0; r < 4; ++r) {
        float v = zs[r];
        v += __shfl_xor(v, 8); v += __shfl_xor(v, 16); v += __shfl_xor(v, 32);
        zs[r] = v;
      }
      if (l < 8) *(AS3 f32x4*)(L + L_ZR + (u32)(((hd * 2 + hf) * 32 + l * 4) * 4)) = zs;
    }
    WAIT_L0();
    BAR();
    {
      u32x4 o0, o1;
#pragma unroll
      for (int pi = 0; pi < 2; ++pi) {
        const int hd = 2 * cg + pi;
        const u32 ga = L_SCR + (u32)(hd * 4096 + l * 32);
        const f32x4 kp = lds128f(L, ga) + lds128f(L, ga + 2048u), kn = lds128f(L, ga + 16u) + lds128f(L, ga + 2064u);
        const u32 za = L_ZR + (u32)((hd * 64 + 4 * gq) * 4);
        const f32x4 zp = lds128f(L, za) + lds128f(L, za + 128u), zn = lds128f(L, za + 64u) + lds128f(L, za + 192u);
        const u32x4 kf = {pk2(kp[0], kp[1]), pk2(kp[2], kp[3]), pk2(kn[0], kn[1]), pk2(kn[2], kn[3])};
        const u32x4 zf = {pk2(zp[0], zp[1]), pk2(zp[2], zp[3]), pk2(zn[0], zn[1]), pk2(zn[2], zn[3])};
#pragma unroll
        for (int rt = 0; rt < 2; ++rt) {
          const f32x4 num = mfma32(kf, phq[pi][rt], zero4), den = mfma32(zf, phq[pi][rt], zero4);
          const float rd = __builtin_amdgcn_rcpf(den[0] + 1e-6f);
          const u32 a = pk2(num[0] * rd, num[1] * rd), bq = pk2(num[2] * rd, num[3] * rd);
          if (rt == 0) { o0[2 * pi] = a; o0[2 * pi + 1] = bq; } else { o1[2 * pi] = a; o1[2 * pi + 1] = bq; }
        }
      }
      const u32 off = WS_AF + ((u32)(8 * p + 2 * rg) * 72u + (u32)(56 + 2 * g + cg)) * 1024u + (u32)l * 16u;
      st_sc1_x4(ws, off, o0);
      st_sc1_x4(ws, off + 72u * 1024u, o1);
    }
    WAIT_V(0);
    BAR();
    if (threadIdx.x == 0) st_sc1_x1(ws, WS_OFLAG + (u32)p * 128u + (u32)g * 4u, tag);
    STAMP(7);
  }

  {
    const int dp = rb >> 1;
    u32x4 xres, b2v;
    const u32 fo = (u32)(64 * rb + (threadIdx.x >> 3)) * 1024u + (u32)(cb * 128 + (threadIdx.x & 7) * 16);
    if (w == 0) poll_flags(ws, WS_HFLAG + (u32)dp * 128u, 8, l, tag);
    BAR();
    STAMP(8);
    const char* af = ws + WS_AF + (size_t)(4 * rb) * 73728u;
    const char* afh = af + (size_t)(7 * w) * 1024u;
    const char* afo = af + (size_t)(56 + 2 * w) * 1024u;
    const u32 vo = (u32)l * 16u;
    f32x4 dacc[4][4];
#pragma unroll
    for (int i = 0; i < 4; ++i)
#pragma unroll
      for (int j = 0; j < 4; ++j) dacc[i][j] = f32x4{0, 0, 0, 0};
    u32x4 a[9][4];
    auto loadA = [&](int j) {
      const char* s = j < 7 ? afh + j * 1024 : afo + (j - 7) * 1024;
#pragma unroll
      for (int rt = 0; rt < 4; ++rt) ld_sc1_x4(a[j][rt], s + rt * 73728, vo);
    };
    auto waitA = [&](int j, auto n) { wait4<decltype(n)::value>(a[j][0], a[j][1], a[j][2], a[j][3]); };
    auto step = [&](int j) {
#pragma unroll
      for (int ct = 0; ct < 4; ++ct) {
        const u32x4 bf = cat(blo[j & 3][ct], bhi[j & 3][ct]);
#pragma unroll
        for (int rt = 0; rt < 4; ++rt) dacc[rt][ct] = mfma32(bf, a[j][rt], dacc[rt][ct]);
      }
      SCHED();
    };
    loadA(0); loadA(1); loadA(2); loadA(3);
    ld_x4(xres, x, fo);
    waitA(0, IC<13>{}); step(0); rdB(IC<4>{}); loadA(4);
    waitA(1, IC<13>{}); waitb(blo[0], bhi[0]); issueB(8); step(1); rdB(IC<5>{}); loadA(5);
    waitA(2, IC<17>{}); step(2); rdB(IC<6>{}); loadA(6);
    waitA(3, IC<17>{}); step(3); rdB(IC<7>{});
    waitA(4, IC<12>{}); step(4);
    waitA(5, IC<4>{}); waitb(blo[1], bhi[1]); step(5); rdB(IC<8>{});
    waitA(6, IC<0>{}); waitb(blo[2], bhi[2]); step(6);
    STAMP(9);
    poll_flags(ws, WS_OFLAG + (u32)dp * 128u + (u32)w * 4u, 1, l, tag);
    STAMP(10);
    loadA(7); loadA(8);
    ld_x4(b2v, b2, (u32)(cb * 128 + (threadIdx.x & 7) * 16));
    waitA(7, IC<5>{}); waitb(blo[3], bhi[3]); step(7);
    waitA(8, IC<1>{}); waitb(blo[0], bhi[0]); step(8);
    WAIT_L0();
#pragma unroll
    for (int rt = 0; rt < 4; ++rt)
#pragma unroll
      for (int ct = 0; ct < 4; ++ct)
        *(AS3 f32x4*)(L + dslot(rt) + (u32)(r16 * 256 + (((ct * 4 + gq) ^ r16) * 16))) = dacc[rt][ct];
    WAIT_L0();
    BAR();
    STAMP(11);
    {
      const int row = threadIdx.x >> 3, cq = threadIdx.x & 7, rt = row >> 4, tk = row & 15;
      const u32 pb = (u32)((rt + 3) & 3) * 32768u + (u32)(tk * 256);
      f32x4 s0 = {0, 0, 0, 0}, s1 = {0, 0, 0, 0};
#pragma unroll
      for (int ww = 0; ww < 8; ++ww) {
        s0 += lds128f(L, pb + (u32)(ww * 4096) + (u32)(((2 * cq) ^ tk) * 16));
        s1 += lds128f(L, pb + (u32)(ww * 4096) + (u32)(((2 * cq + 1) ^ tk) * 16));
      }
      asm volatile("s_waitcnt vmcnt(0)" : "+v"(xres), "+v"(b2v) :: "memory");
      u32x4 o;
      o[0] = pk2(s0[0] + bflo(xres[0]) + bflo(b2v[0]), s0[1] + bfhi(xres[0]) + bfhi(b2v[0]));
      o[1] = pk2(s0[2] + bflo(xres[1]) + bflo(b2v[1]), s0[3] + bfhi(xres[1]) + bfhi(b2v[1]));
      o[2] = pk2(s1[0] + bflo(xres[2]) + bflo(b2v[2]), s1[1] + bfhi(xres[2]) + bfhi(b2v[2]));
      o[3] = pk2(s1[2] + bflo(xres[3]) + bflo(b2v[3]), s1[3] + bfhi(xres[3]) + bfhi(b2v[3]));
      *(u32x4*)(out + fo) = o;
    }
    if (b == 255 && w == 7) {
#pragma unroll 1
      for (int i = 0; i < 4; ++i) poll_flags(ws, WS_AFLAG + (u32)i * 256u, 64, l, tag);
      if (l == 0) st_sc1_x1(ws, WS_EPOCH, tag);
    }
    STAMP(12);
  }
}

extern "C" void kernel_launch(void* const* d_in, const int* in_sizes, int n_in, void* d_out, int out_size, void* d_ws,
                              size_t ws_size, hipStream_t stream) {
  (void)in_sizes; (void)n_in; (void)out_size; (void)ws_size;
  hipFuncSetAttribute((const void*)fused_kernel, hipFuncAttributeMaxDynamicSharedMemorySize, LDS_BYTES);
  fused_kernel<<<256, 512, LDS_BYTES, stream>>>((const char*)d_in[0], (const char*)d_in[1], (const char*)d_in[2],
                                                (const char*)d_in[3], (const char*)d_in[5], (const char*)d_in[6],
                                                (const char*)d_in[7], (const char*)d_in[8], (char*)d_out, (char*)d_ws);
}
```

```cpp
#include <hip/hip_runtime.h>
#include <stdint.h>

#define TIMING 1
#include <stdio.h>
#define EXP_GL 1
typedef unsigned int u32;
typedef unsigned long long u64;
typedef __bf16 bf16x8 __attribute__((ext_vector_type(8)));
typedef __bf16 bf16x2 __attribute__((ext_vector_type(2)));
typedef short v4i16 __attribute__((ext_vector_type(4)));
typedef float f32x4 __attribute__((ext_vector_type(4)));
typedef float f32x2 __attribute__((ext_vector_type(2)));
typedef u32 u32x4 __attribute__((ext_vector_type(4)));
typedef u32 u32x2 __attribute__((ext_vector_type(2)));

#define AS1 __attribute__((address_space(1)))
#define AS3 __attribute__((address_space(3)))
#define AS4 __attribute__((address_space(4)))
#define DEVI __device__ __forceinline__

constexpr u32 WS_EPOCH = 0;
constexpr u32 WS_AFLAG = 1024;
constexpr u32 WS_HFLAG = 2048;
constexpr u32 WS_OFLAG = 4096;
constexpr u32 WS_KVFLAG = 6144;
constexpr u32 WS_STAT = 8192;
constexpr u32 WS_CS = 40960;
constexpr u32 WS_KVP = 0x50000;
constexpr u32 WS_ZP = 0xD0000;
constexpr u32 WS_AF = 0x100000;
constexpr u32 WS_TIM = 0xB00000;
constexpr u32 WS_USED = 0xC00000;

constexpr u32 SLOT = 49152;
constexpr u32 L_SCR = 131072;
constexpr u32 L_ZR = L_SCR + 24576;
constexpr u32 L_CSB = 157696;
constexpr u32 L_MEAN = 159744;
constexpr u32 L_RSTD = 160256;
constexpr u32 L_WPHI = 160768;
constexpr u32 LDS_BYTES = 162816;

DEVI f32x4 mfma32(u32x4 a, u32x4 b, f32x4 c) {
  return __builtin_amdgcn_mfma_f32_16x16x32_bf16(__builtin_bit_cast(bf16x8, a), __builtin_bit_cast(bf16x8, b), c, 0, 0, 0);
}
DEVI f32x4 mfma16(u32x2 a, u32x2 b, f32x4 c) {
  return __builtin_amdgcn_mfma_f32_16x16x16bf16_1k(__builtin_bit_cast(v4i16, a), __builtin_bit_cast(v4i16, b), c, 0, 0, 0);
}
DEVI u32 pk2(float a, float b) {
  f32x2 v = {a, b};
  return __builtin_bit_cast(u32, __builtin_convertvector(v, bf16x2));
}
DEVI float bflo(u32 v) { return __builtin_bit_cast(float, v << 16); }
DEVI float bfhi(u32 v) { return __builtin_bit_cast(float, v & 0xffff0000u); }
DEVI u32 fbits(float f) { return __builtin_bit_cast(u32, f); }
DEVI float bitsf(u32 u) { return __builtin_bit_cast(float, u); }

DEVI u32x2 lds_tr(AS3 char* L, u32 addr) {
  v4i16 t = __builtin_amdgcn_ds_read_tr16_b64_v4i16((AS3 v4i16*)(L + addr));
  return __builtin_bit_cast(u32x2, t);
}
DEVI u32x4 lds_frag_tr(AS3 char* L, u32 addr, u32 second) {
  u32x2 a = lds_tr(L, addr), b = lds_tr(L, addr + second);
  u32x4 r = {a[0], a[1], b[0], b[1]};
  return r;
}
DEVI u32x4 lds128(AS3 char* L, u32 addr) { return *(AS3 u32x4*)(L + addr); }
DEVI f32x4 lds128f(AS3 char* L, u32 addr) { return *(AS3 f32x4*)(L + addr); }
template <int OFF> DEVI void glds_m0(u32 m0v, const char* sbase, u32 voff) {
  asm volatile("s_mov_b32 m0, %0\n\ts_nop 0\n\tglobal_load_lds_dwordx4 %1, %2 offset:%3" ::"s"(m0v), "v"(voff), "s"(sbase), "n"(OFF) : "memory");
}
template <int OFF> DEVI void glds(const char* sbase, u32 voff) {
  asm volatile("global_load_lds_dwordx4 %0, %1 offset:%2" ::"v"(voff), "s"(sbase), "n"(OFF) : "memory");
}
template <int OFF> DEVI void glds_masked(const char* sbase, u32 voff, u64 mask) {
  asm volatile("s_mov_b64 exec, %3\n\tglobal_load_lds_dwordx4 %0, %1 offset:%2\n\ts_mov_b64 exec, -1" ::"v"(voff), "s"(sbase), "n"(OFF), "s"(mask) : "memory");
}
template <int OFF> DEVI void dsr128(u32x4& d, u32 a) {
  asm volatile("ds_read_b128 %0, %1 offset:%2" : "=v"(d) : "v"(a), "n"(OFF) : "memory");
}
template <int OFF> DEVI void dsrtr(u32x2& d, u32 a) {
  asm volatile("ds_read_b64_tr_b16 %0, %1 offset:%2" : "=v"(d) : "v"(a), "n"(OFF) : "memory");
}
DEVI u32x4 cat(u32x2 a, u32x2 b) { u32x4 r = {a[0], a[1], b[0], b[1]}; return r; }
#define WAIT_V(n) asm volatile("s_waitcnt vmcnt(%0)" ::"n"(n) : "memory")
#define WAIT_L0() asm volatile("s_waitcnt lgkmcnt(0)" ::: "memory")
#define BAR() __builtin_amdgcn_s_barrier()
#define SCHED() __builtin_amdgcn_sched_barrier(0)

DEVI void ld_sc1_x4(u32x4& d, const char* base, u32 off) {
  asm volatile("global_load_dwordx4 %0, %1, %2 sc1" : "=v"(d) : "v"(off), "s"(base) : "memory");
}
DEVI void ld_x4(u32x4& d, const char* base, u32 off) {
  asm volatile("global_load_dwordx4 %0, %1, %2" : "=v"(d) : "v"(off), "s"(base) : "memory");
}
DEVI void ld_x1(u32& d, const char* base, u32 off) {
  asm volatile("global_load_dword %0, %1, %2" : "=v"(d) : "v"(off), "s"(base) : "memory");
}
DEVI void ld_u16(u32& d, const char* base, u32 off) {
  asm volatile("global_load_ushort %0, %1, %2" : "=v"(d) : "v"(off), "s"(base) : "memory");
}
DEVI u32 ld_sc1_x1_wait(const char* base, u32 off) {
  u32 d;
  asm volatile("global_load_dword %0, %1, %2 sc1\n\ts_waitcnt vmcnt(0)" : "=v"(d) : "v"(off), "s"(base) : "memory");
  return d;
}
DEVI void st_sc1_x4(char* base, u32 off, u32x4 v) {
  asm volatile("global_store_dwordx4 %0, %1, %2 sc1\n\ts_nop 1" ::"v"(off), "v"(v), "s"(base) : "memory");
}
DEVI void st_sc1_x2(char* base, u32 off, u32x2 v) {
  asm volatile("global_store_dwordx2 %0, %1, %2 sc1" ::"v"(off), "v"(v), "s"(base) : "memory");
}
DEVI void st_sc1_x1(char* base, u32 off, u32 v) {
  asm volatile("global_store_dword %0, %1, %2 sc1" ::"v"(off), "v"(v), "s"(base) : "memory");
}
constexpr int SPIN_MAX = 1 << 17;
DEVI void poll_flags(const char* base, u32 off0, int n, int l, u32 tag) {
  u32 off = off0 + (u32)(l < n ? l : 0) * 4u;
#pragma unroll 1
  for (int it = 0; it < SPIN_MAX; ++it) {
    u32 v = ld_sc1_x1_wait(base, off);
    if (__builtin_amdgcn_ballot_w64(v != tag) == 0ull) break;
    __builtin_amdgcn_s_sleep(1);
  }
  __builtin_amdgcn_fence(__ATOMIC_ACQUIRE, "wavefront");
}

#define STAMPX(i) asm volatile("s_memrealtime %0\n\ts_waitcnt lgkmcnt(0)" : "=s"(ts[i]) :: "memory")
#define STAMP(i) STAMPX(i)
#define STAMPG(i) do {} while (0)
#define STAMP_DECL u64 ts[16] = {0, 0, 0, 0, 0, 0, 0, 0, 0, 0, 0, 0, 0, 0, 0, 0}; u64 tc0, tc1; \
  asm volatile("s_memtime %0\n\ts_waitcnt lgkmcnt(0)" : "=s"(tc0) :: "memory")
#define STAMP_FLUSH do { asm volatile("s_memtime %0\n\ts_waitcnt lgkmcnt(0)" : "=s"(tc1) :: "memory"); ts[15] = tc1 - tc0; \
  if (l == 0) { _Pragma("unroll") for (int _i = 0; _i < 16; ++_i) *(u64*)(ws + WS_TIM + ((b * 8 + w) * 32 + _i) * 8) = ts[_i]; } } while (0)

DEVI void red4_max2(float& a, float& b) {
  auto s = __builtin_amdgcn_permlane32_swap(fbits(a), fbits(b), false, false);
  float t = fmaxf(bitsf(s[0]), bitsf(s[1]));
  auto u = __builtin_amdgcn_permlane16_swap(fbits(t), fbits(t), false, false);
  float m = fmaxf(bitsf(u[0]), bitsf(u[1]));
  auto z = __builtin_amdgcn_permlane32_swap(fbits(m), fbits(m), false, false);
  a = bitsf(z[0]); b = bitsf(z[1]);
}
DEVI void red4_sum2_rcp(float& a, float& b) {
  auto s = __builtin_amdgcn_permlane32_swap(fbits(a), fbits(b), false, false);
  float t = bitsf(s[0]) + bitsf(s[1]);
  auto u = __builtin_amdgcn_permlane16_swap(fbits(t), fbits(t), false, false);
  float m = __builtin_amdgcn_rcpf(bitsf(u[0]) + bitsf(u[1]));
  auto z = __builtin_amdgcn_permlane32_swap(fbits(m), fbits(m), false, false);
  a = bitsf(z[0]); b = bitsf(z[1]);
}
DEVI u32x4 dual_softmax(f32x4 p) {
  const float L2E = 1.4426950408889634f;
  float mx = fmaxf(fmaxf(p[0], p[1]), fmaxf(p[2], p[3]));
  float nm = fmaxf(fmaxf(-p[0], -p[1]), fmaxf(-p[2], -p[3]));
  red4_max2(mx, nm);
  float a = -mx * L2E, c = -nm * L2E;
  float e0 = __builtin_amdgcn_exp2f(fmaf(p[0], L2E, a)), e1 = __builtin_amdgcn_exp2f(fmaf(p[1], L2E, a));
  float e2 = __builtin_amdgcn_exp2f(fmaf(p[2], L2E, a)), e3 = __builtin_amdgcn_exp2f(fmaf(p[3], L2E, a));
  float n0 = __builtin_amdgcn_exp2f(fmaf(p[0], -L2E, c)), n1 = __builtin_amdgcn_exp2f(fmaf(p[1], -L2E, c));
  float n2 = __builtin_amdgcn_exp2f(fmaf(p[2], -L2E, c)), n3 = __builtin_amdgcn_exp2f(fmaf(p[3], -L2E, c));
  float sp = (e0 + e1) + (e2 + e3), sn = (n0 + n1) + (n2 + n3);
  red4_sum2_rcp(sp, sn);
  u32x4 r = {pk2(e0 * sp, e1 * sp), pk2(e2 * sp, e3 * sp), pk2(n0 * sn, n1 * sn), pk2(n2 * sn, n3 * sn)};
  return r;
}

template <int V> struct IC { static constexpr int value = V; };
template <int N> DEVI void wait4(u32x4& a, u32x4& b, u32x4& c, u32x4& d) {
  asm volatile("s_waitcnt vmcnt(%4)" : "+v"(a), "+v"(b), "+v"(c), "+v"(d) : "n"(N) : "memory");
}
template <int N> DEVI void wait1(u32x4& a) { asm volatile("s_waitcnt vmcnt(%1)" : "+v"(a) : "n"(N) : "memory"); }

template <int N> DEVI void waitset(u32x4& a, u32x4& b, u32x2 (&lo)[8], u32x2 (&hi)[8]);
template <> DEVI void waitset<8>(u32x4& a, u32x4& b, u32x2 (&lo)[8], u32x2 (&hi)[8]) {
  asm volatile("s_waitcnt lgkmcnt(0)"
               : "+v"(a), "+v"(b), "+v"(lo[0]), "+v"(lo[1]), "+v"(lo[2]), "+v"(lo[3]), "+v"(lo[4]), "+v"(lo[5]), "+v"(lo[6]), "+v"(lo[7]),
                 "+v"(hi[0]), "+v"(hi[1]), "+v"(hi[2]), "+v"(hi[3]), "+v"(hi[4]), "+v"(hi[5]), "+v"(hi[6]), "+v"(hi[7])
               :: "memory");
}
template <> DEVI void waitset<6>(u32x4& a, u32x4& b, u32x2 (&lo)[8], u32x2 (&hi)[8]) {
  asm volatile("s_waitcnt lgkmcnt(0)"
               : "+v"(a), "+v"(b), "+v"(lo[0]), "+v"(lo[1]), "+v"(lo[2]), "+v"(lo[3]), "+v"(lo[4]), "+v"(lo[5]),
                 "+v"(hi[0]), "+v"(hi[1]), "+v"(hi[2]), "+v"(hi[3]), "+v"(hi[4]), "+v"(hi[5])
               :: "memory");
}
DEVI void waitb(u32x2 (&lo)[4], u32x2 (&hi)[4]) {
  asm volatile("s_waitcnt lgkmcnt(0)"
               : "+v"(lo[0]), "+v"(lo[1]), "+v"(lo[2]), "+v"(lo[3]), "+v"(hi[0]), "+v"(hi[1]), "+v"(hi[2]), "+v"(hi[3]) :: "memory");
}
DEVI float wave_sum(float v) {
#define DPP_ADD(ctrl, rmask) v += bitsf((u32)__builtin_amdgcn_update_dpp(0, (int)fbits(v), ctrl, rmask, 0xf, false))
  DPP_ADD(0x111, 0xf); DPP_ADD(0x112, 0xf); DPP_ADD(0x114, 0xf); DPP_ADD(0x118, 0xf);
  DPP_ADD(0x142, 0xa); DPP_ADD(0x143, 0xc);
#undef DPP_ADD
  return bitsf((u32)__builtin_amdgcn_readlane((int)fbits(v), 63));
}

extern "C" __global__ void __launch_bounds__(512, 2)
fused_kernel(const char* __restrict__ x, const char* __restrict__ wqkv, const char* __restrict__ wphi,
             const char* __restrict__ wo, const char* __restrict__ w1, const char* __restrict__ b1,
             const char* __restrict__ w2, const char* __restrict__ b2, char* __restrict__ out, char* __restrict__ ws) {
  extern __shared__ __attribute__((aligned(16))) char smem[];
  AS3 char* L = (AS3 char*)smem;
  asm volatile("" ::"s"(x), "s"(wqkv), "s"(wphi), "s"(wo), "s"(w1), "s"(b1), "s"(w2), "s"(b2), "s"(out), "s"(ws));
  const int b = blockIdx.x;
  const int l = threadIdx.x & 63;
  const int w = __builtin_amdgcn_readfirstlane(threadIdx.x >> 6);
  const int xcd = b & 7, bi = b >> 3;
  const int p = (xcd >> 2) * 8 + (bi >> 2), c = (xcd & 3) * 4 + (bi & 3), g = c >> 1;
  const bool typeA = (c & 1) == 0;
  const int rg = w & 3, cg = w >> 2;
  const int gq = l >> 4, r16 = l & 15, q4 = (l >> 2) & 3, pp = l & 3;
  const int rot = (p + c) & 7;
  const int rb = 4 * xcd + (bi >> 3), cb = bi & 7;
  STAMP_DECL;
  STAMP(0);

  const u32 lb = (u32)(size_t)L;
  const u32 xrow_off = (u32)(128 * p + 8 * c + w) * 1024u + (u32)l * 16u;
  u32 gb1[2];
  const u32 gb1_off = (u32)(224 * g + (w < 6 ? 32 * w + ((g & 1) ? 32 : 0) : (w == 6 && !(g & 1) ? 192 : 0)) + (l >> 2)) * 2u;
  u32 tag;

  const char* xsrc = x + (size_t)p * 131072u;
  const u32 wstride = typeA ? 3072u : 3584u;
  const char* wsrc = (typeA ? (wqkv + g * 128) : (w1 + 448 * g)) - 4096;
  const u32 vx0 = (u32)(16 * w + (l >> 3)) * 1024u + (u32)(((l & 7) ^ (l >> 4)) * 16);
  const u32 vx1 = (u32)(16 * w + 7 + (l >> 3)) * 1024u + (u32)(((l & 7) ^ (4 + (l >> 4))) * 16);
  const u32 wsp = (u32)(((l & 7) >> 1) ^ (((l >> 4) & 1) | ((w & 1) << 1)));
  const u32 vw = (u32)(8 * w + (l >> 3)) * wstride + (wsp * 2u + (u32)(l & 1)) * 16u + 4096u;
  u32 vws[4];
  {
    const u32 odd = (u32)(g & 1) * 64u;
    vws[0] = typeA ? vw : vw + odd;
    vws[1] = typeA ? vw : vw + odd + 128u - 1024u;
    vws[2] = typeA ? vw : vw + odd + 256u - 2048u;
    vws[3] = vw + (odd ? 0u : 384u) - 3072u;
  }
  const u64 hmask = __builtin_amdgcn_ballot_w64(wsp < 2u);
  const u32 m0x = lb + (u32)w * 2048u, m0w = lb + 16384u + (u32)w * 4096u;

  const char* dsrc2 = w2 + (size_t)(7 * w) * 32768u + cb * 128;
  const char* dsrco = wo + (size_t)(2 * w) * 32768u + cb * 128;
  const u32 vdb = (u32)(l >> 3) * 1024u + (u32)(((((l & 7) >> 1) ^ ((l >> 4) & 3)) * 2 + (l & 1)) * 16);
  auto dslot = [&](int j) -> u32 { return (u32)((j + 3) & 3) * 32768u + (u32)w * 4096u; };
  auto issueB = [&](int j) {
    const char* s = (j < 7) ? (dsrc2 + j * 32768) : (dsrco + (j - 7) * 32768);
    glds_m0<0>(lb + dslot(j), s, vdb);
    glds<1024>(s, vdb + 7168u);
    glds<2048>(s, vdb + 2u * 7168u);
    glds<3072>(s, vdb + 3u * 7168u);
  };

  u32 xa0 = lb + (u32)(32 * rg + r16) * 128u + (u32)((gq ^ ((r16 >> 1) & 7)) * 16);
  u32 xa1 = xa0 ^ 64u;
  const u32 vbase = lb + 16384u + (u32)gq * 4096u + (u32)q4 * 128u + (u32)pp * 8u + (u32)(((q4 >> 1) & 1) | ((gq & 1) << 1)) * 32u;
  u32 R[8];
#pragma unroll
  for (int t = 0; t < 8; ++t) {
    const u32 ra = (vbase ^ (u32)((2 * cg + (t & 1)) * 32)) + (u32)(t >> 1) * 1024u;
    const u32 rbb = (vbase ^ (u32)((t & 3) * 32)) + (u32)(t >> 2) * 1024u + (u32)cg * 2048u;
    R[t] = typeA ? ra : rbb;
  }

  f32x4 acc[8][2];
#pragma unroll
  for (int t = 0; t < 8; ++t) { acc[t][0] = f32x4{0, 0, 0, 0}; acc[t][1] = f32x4{0, 0, 0, 0}; }
  u32x4 gat[8];
  const u32x4 ones = {0x3F803F80u, 0x3F803F80u, 0x3F803F80u, 0x3F803F80u};
  const u32 gcs = WS_CS + (u32)(c * 256 + 32 * w + (l >> 2)) * 64u + (u32)(l & 3) * 16u;
  const u32 gst = WS_STAT + (u32)(128 * p + 16 * w + r16) * 16u;

  auto g1 = [&](auto TAc, auto N8c) {
    constexpr bool TA = decltype(TAc)::value != 0, N8 = decltype(N8c)::value != 0;
    constexpr int NG = TA ? 5 : 6;
    u32x4 fx[2][2];
    u32x2 flo[2][8], fhi[2][8];
    u32x4 xrow;
    u32 epoch;
    asm volatile("s_load_dword %0, %1, 0x0" : "=s"(epoch) : "s"(ws) : "memory");
    ld_x4(xrow, x, xrow_off);
    if (TA) { gb1[0] = 0; gb1[1] = 0; }
    else { ld_u16(gb1[0], b1, gb1_off); ld_u16(gb1[1], b1, gb1_off + 32u); }
    auto issue = [&](int s, u32 sb) {
      const int ks = (s + rot) & 7;
      const char* xs = xsrc + ks * 128;
      const char* wsb = wsrc + (size_t)((u32)ks * 64u * wstride);
      glds_m0<0>(m0x + sb, xs, vx0);
      glds<1024>(xs, vx1);
      glds_m0<0>(m0w + sb, wsb, vws[0]);
      glds<1024>(wsb, vws[1]);
      glds<2048>(wsb, vws[2]);
      if (!TA) glds_masked<3072>(wsb, vws[3], hmask);
    };
    issue(0, 0); issue(1, SLOT); issue(2, 2 * SLOT);
    if (TA && w < 2) glds_m0<0>(lb + L_WPHI + (u32)w * 1024u, wphi + g * 2048 + w * 1024, (u32)l * 16u);

#define MF(S, t, rt) acc[t][rt] = mfma32(cat(flo[S][t], fhi[S][t]), fx[S][rt], acc[t][rt])
#define MFS(S, t, rt) acc[t][rt] = mfma32(fx[S][rt], cat(flo[S][t], fhi[S][t]), acc[t][rt])
#define RW(N, t) do { if (RD) { dsrtr<(N) * 16384>(flo[N][t], R[t]); dsrtr<(N) * 16384 + 512>(fhi[N][t], R[t]); } } while (0)
#define RX(N) do { if (RD) { dsr128<0>(fx[N][0], (N) ? xa1 : xa0); dsr128<2048>(fx[N][1], (N) ? xa1 : xa0); } } while (0)
    auto waitS = [&](auto Sc) {
      constexpr int S = decltype(Sc)::value;
      if constexpr (N8) waitset<8>(fx[S][0], fx[S][1], flo[S], fhi[S]);
      else waitset<6>(fx[S][0], fx[S][1], flo[S], fhi[S]);
    };
    auto phase = [&](auto Sc, auto RDc, auto GLc, const char* xs, const char* wsb, u32 sb) {
      constexpr int S = decltype(Sc)::value, N = 1 - S;
      constexpr bool RD = decltype(RDc)::value != 0, GL = decltype(GLc)::value != 0;
      waitS(Sc);
      if (S == 1 && RD) BAR();
      SCHED();
      if constexpr (N8) {
        MF(S, 6, 0); RW(N, 6); SCHED();
        MF(S, 6, 1); RW(N, 7); SCHED();
        MF(S, 7, 0); SCHED();
        MF(S, 7, 1); SCHED();
      }
      if constexpr (TA) {
        MFS(S, 4, 0); RX(N); SCHED();
        MFS(S, 4, 1); RW(N, 4); SCHED();
        MFS(S, 5, 0); RW(N, 5); SCHED();
        MFS(S, 5, 1); RW(N, 0); SCHED();
      } else {
        MF(S, 4, 0); RX(N); SCHED();
        MF(S, 4, 1); RW(N, 4); SCHED();
        MF(S, 5, 0); RW(N, 5); SCHED();
        MF(S, 5, 1); RW(N, 0); SCHED();
      }
      MF(S, 0, 0); RW(N, 1); SCHED();
      MF(S, 0, 1); RW(N, 2); SCHED();
      MF(S, 1, 0); RW(N, 3); SCHED();
      MF(S, 1, 1); if (GL) { glds_m0<0>(m0x + sb, xs, vx0); glds<1024>(xs, vx1); } SCHED();
      MF(S, 2, 0); if (GL) glds_m0<0>(m0w + sb, wsb, vws[0]); SCHED();
      MF(S, 2, 1); if (GL) glds<1024>(wsb, vws[1]); SCHED();
      MF(S, 3, 0); if (GL) glds<2048>(wsb, vws[2]); SCHED();
      MF(S, 3, 1); if (GL && !TA) glds_masked<3072>(wsb, vws[3], hmask); SCHED();
    };
    auto advance = [&](u32 d) {
#pragma unroll
      for (int t = 0; t < (N8 ? 8 : 6); ++t) R[t] += d;
      xa0 += d; xa1 += d;
    };

    STAMP(1);
    if (TA) asm volatile("s_waitcnt vmcnt(%1)" : "+v"(xrow) : "n"(2 * NG) : "memory");
    else asm volatile("s_waitcnt vmcnt(%3)" : "+v"(xrow), "+v"(gb1[0]), "+v"(gb1[1]) : "n"(2 * NG) : "memory");
    STAMPG(1);
    BAR();
    STAMPG(2);
    {
      constexpr bool RD = true;
      RX(0); RW(0, 4); RW(0, 5); RW(0, 0); RW(0, 1); RW(0, 2); RW(0, 3);
      if constexpr (N8) { RW(0, 6); RW(0, 7); }
    }
    waitS(IC<0>{});
    asm volatile("" : "+s"(epoch) :: "memory");
    tag = epoch + 1u;
    phase(IC<0>{}, IC<1>{}, IC<0>{}, nullptr, nullptr, 0u);
    {
      float v[8];
#pragma unroll
      for (int i = 0; i < 4; ++i) { v[2 * i] = bflo(xrow[i]); v[2 * i + 1] = bfhi(xrow[i]); }
      const float mean = wave_sum(((v[0] + v[1]) + (v[2] + v[3])) + ((v[4] + v[5]) + (v[6] + v[7]))) * (1.0f / 512.0f);
      float qq = 0.f;
#pragma unroll
      for (int i = 0; i < 8; ++i) { float d = v[i] - mean; qq = fmaf(d, d, qq); }
      const float rstd = 1.0f / sqrtf(wave_sum(qq) * (1.0f / 512.0f) + 1e-5f);
      if (l == 0) {
        u32x4 gr = {fbits(mean), fbits(rstd), tag, tag};
        st_sc1_x4(ws, WS_STAT + (u32)(128 * p + 8 * c + w) * 16u, gr);
        if (w == 7) st_sc1_x1(ws, WS_AFLAG + (u32)b * 4u, tag);
      }
      if (p < 8 && w < (TA ? 6 : 7)) {
        u32x2 c0l[2], c0h[2], c1l[2], c1h[2];
        const u32 a0 = (vbase ^ (u32)((2 * (w & 1)) * 32)) + (u32)(w >> 1) * 1024u;
        const u32 a1 = a0 ^ 32u;
        dsrtr<0>(c0l[0], a0); dsrtr<512>(c0h[0], a0); dsrtr<16384>(c0l[1], a0); dsrtr<16384 + 512>(c0h[1], a0);
        dsrtr<0>(c1l[0], a1); dsrtr<512>(c1h[0], a1); dsrtr<16384>(c1l[1], a1); dsrtr<16384 + 512>(c1h[1], a1);
        asm volatile("s_waitcnt lgkmcnt(0)" : "+v"(c0l[0]), "+v"(c0h[0]), "+v"(c0l[1]), "+v"(c0h[1]),
                     "+v"(c1l[0]), "+v"(c1h[0]), "+v"(c1l[1]), "+v"(c1h[1]) :: "memory");
        f32x4 c0 = {0, 0, 0, 0}, c1 = {0, 0, 0, 0};
#pragma unroll
        for (int ks = 0; ks < 2; ++ks) {
          c0 = mfma32(ones, cat(c0l[ks], c0h[ks]), c0);
          c1 = mfma32(ones, cat(c1l[ks], c1h[ks]), c1);
        }
        if (l < 32) {
          u32x2 gr = {fbits(l < 16 ? c0[0] : c1[0]), tag};
          st_sc1_x2(ws, WS_CS + (u32)((c * 256 + 32 * w + l) * 8 + rot) * 8u, gr);
        }
      }
    }
    u32 sb = 0;
#pragma unroll 1
    for (int s = 0; s < 5; ++s) {
      const u32 nsb = sb == 2 * SLOT ? 0u : sb + SLOT;
      advance(nsb - sb);
      const int ks = (s + 3 + rot) & 7;
      WAIT_V(NG);
      STAMPG(3 + s);
      phase(IC<1>{}, IC<1>{}, IC<EXP_GL>{}, xsrc + ks * 128, wsrc + (size_t)((u32)ks * 64u * wstride), sb);
      phase(IC<0>{}, IC<1>{}, IC<0>{}, nullptr, nullptr, 0u);
      sb = nsb;
    }
    advance(0u - 2u * SLOT);
    WAIT_V(NG);
    STAMPG(8);
    phase(IC<1>{}, IC<1>{}, IC<0>{}, nullptr, nullptr, 0u);
    ld_sc1_x4(gat[0], ws, gcs);
    ld_sc1_x4(gat[1], ws, gcs + 1024u);
    ld_sc1_x4(gat[2], ws, gst);
    issueB(0);
    phase(IC<0>{}, IC<1>{}, IC<0>{}, nullptr, nullptr, 0u);
    advance(SLOT);
    WAIT_V(7);
    STAMPG(9);
    phase(IC<1>{}, IC<1>{}, IC<0>{}, nullptr, nullptr, 0u);
    issueB(1);
    phase(IC<0>{}, IC<1>{}, IC<0>{}, nullptr, nullptr, 0u);
    phase(IC<1>{}, IC<0>{}, IC<0>{}, nullptr, nullptr, 0u);
#undef MF
#undef MFS
#undef RW
#undef RX
    STAMP(2);
    STAMPG(10);
    asm volatile("s_waitcnt vmcnt(8)" : "+v"(gat[0]), "+v"(gat[1]), "+v"(gat[2]) :: "memory");
    STAMPG(11);
  };
  if (typeA) g1(IC<1>{}, IC<0>{});
  else if (cg == 0) g1(IC<0>{}, IC<1>{});
  else g1(IC<0>{}, IC<0>{});
  {
    const bool used = w < (typeA ? 6 : 7);
  #pragma unroll 1
  for (int it = 0; it < SPIN_MAX; ++it) {
      const bool bad = (used && ((gat[0][1] != tag) | (gat[0][3] != tag) | (gat[1][1] != tag) | (gat[1][3] != tag))) | (gat[2][2] != tag);
      if (__builtin_amdgcn_ballot_w64(bad) == 0ull) break;
      ld_sc1_x4(gat[0], ws, gcs);
      ld_sc1_x4(gat[1], ws, gcs + 1024u);
      ld_sc1_x4(gat[2], ws, gst);
      asm volatile("s_waitcnt vmcnt(0)" : "+v"(gat[0]), "+v"(gat[1]), "+v"(gat[2]) :: "memory");
    }
#pragma unroll
    for (int i = 0; i < 2; ++i) {
      float sm = bitsf(gat[i][0]) + bitsf(gat[i][2]);
      sm += bitsf((u32)__builtin_amdgcn_update_dpp(0, (int)fbits(sm), 0xB1, 0xf, 0xf, false));
      sm += bitsf((u32)__builtin_amdgcn_update_dpp(0, (int)fbits(sm), 0x4E, 0xf, 0xf, false));
      if ((l & 3) == 0) {
        f32x2 o = {sm, bflo(gb1[i])};
        *(AS3 f32x2*)(L + L_CSB + (u32)(32 * w + 16 * i + (l >> 2)) * 8u) = o;
      }
    }
    if (l < 16) {
      *(AS3 float*)(L + L_MEAN + (u32)(16 * w + l) * 4u) = bitsf(gat[2][0]);
      *(AS3 float*)(L + L_RSTD + (u32)(16 * w + l) * 4u) = bitsf(gat[2][1]);
    }
  }
  WAIT_L0();
  BAR();
  issueB(2); issueB(3);
  STAMP(3);

  const float mean0 = *(AS3 float*)(L + L_MEAN + (u32)(32 * rg + r16) * 4u);
  const float mean1 = *(AS3 float*)(L + L_MEAN + (u32)(32 * rg + 16 + r16) * 4u);
  const float rstd0 = *(AS3 float*)(L + L_RSTD + (u32)(32 * rg + r16) * 4u);
  const float rstd1 = *(AS3 float*)(L + L_RSTD + (u32)(32 * rg + 16 + r16) * 4u);

  const u32 vb2 = (u32)(4 * gq + q4) * 128u + (u32)pp * 8u + (u32)((2 * gq + (q4 >> 1)) & 3) * 32u;
  u32x2 blo[4][4], bhi[4][4];
  u32 VB[4];
#pragma unroll
  for (int ct = 0; ct < 4; ++ct) VB[ct] = lb + (vb2 ^ (u32)(ct * 32)) + (u32)w * 4096u;
  auto rdB = [&](auto jc) {
    constexpr int j = decltype(jc)::value, blk = (j + 3) & 3;
#pragma unroll
    for (int ct = 0; ct < 4; ++ct) {
      const u32 a = VB[ct] + (blk >= 2 ? 65536u : 0u);
      dsrtr<(blk & 1) * 32768>(blo[j & 3][ct], a);
      dsrtr<(blk & 1) * 32768 + 2048>(bhi[j & 3][ct], a);
    }
  };
  auto loadBreg = [&]() {
    rdB(IC<0>{}); rdB(IC<1>{}); waitb(blo[0], bhi[0]); waitb(blo[1], bhi[1]);
    rdB(IC<2>{}); rdB(IC<3>{}); waitb(blo[2], bhi[2]); waitb(blo[3], bhi[3]);
    issueB(4); issueB(5); issueB(6); issueB(7);
  };

  if (!typeA) {
    auto hpair = [&](auto Pc) {
      constexpr int P = decltype(Pc)::value;
      const int hl = cg * 4 + P;
      u32x4 o0, o1;
#pragma unroll
      for (int e = 0; e < 2; ++e) {
        const int t = 2 * P + e;
        const u32 ca = L_CSB + (u32)(hl * 32 + e * 16 + 4 * gq) * 8u;
        const f32x4 cb0 = lds128f(L, ca), cb1 = lds128f(L, ca + 16u);
        const float cs[4] = {cb0[0], cb0[2], cb1[0], cb1[2]}, bb[4] = {cb0[1], cb0[3], cb1[1], cb1[3]};
        float h0[4], h1[4];
#pragma unroll
        for (int r = 0; r < 4; ++r) {
          float y0 = fmaf(rstd0, fmaf(-mean0, cs[r], acc[t][0][r]), bb[r]);
          float y1 = fmaf(rstd1, fmaf(-mean1, cs[r], acc[t][1][r]), bb[r]);
          y0 = fmaxf(y0, 0.f); y1 = fmaxf(y1, 0.f);
          h0[r] = y0 * y0; h1[r] = y1 * y1;
        }
        o0[2 * e] = pk2(h0[0], h0[1]); o0[2 * e + 1] = pk2(h0[2], h0[3]);
        o1[2 * e] = pk2(h1[0], h1[1]); o1[2 * e + 1] = pk2(h1[2], h1[3]);
      }
      const u32 step = (u32)(7 * g + ((g & 1) ? (hl < 6 ? hl + 1 : 0) : hl));
      const u32 off = WS_AF + ((u32)(8 * p + 2 * rg) * 72u + step) * 1024u + (u32)l * 16u;
      st_sc1_x4(ws, off, o0);
      st_sc1_x4(ws, off + 72u * 1024u, o1);
    };
    hpair(IC<0>{}); hpair(IC<1>{}); hpair(IC<2>{});
    if (cg == 0) hpair(IC<3>{});
    WAIT_V(0);
    BAR();
    if (threadIdx.x == 0) st_sc1_x1(ws, WS_HFLAG + (u32)p * 128u + (u32)g * 4u, tag);
    STAMP(4);
    loadBreg();
  } else {
    const u32x2 ident = {(4 * gq + 0 == r16 ? 0x3F80u : 0u) | (4 * gq + 1 == r16 ? 0x3F800000u : 0u),
                         (4 * gq + 2 == r16 ? 0x3F80u : 0u) | (4 * gq + 3 == r16 ? 0x3F800000u : 0u)};
    const f32x4 zero4 = {0, 0, 0, 0};
    const f32x4 vm0 = lds128f(L, L_MEAN + (u32)(32 * rg + 4 * gq) * 4u), vm1 = lds128f(L, L_MEAN + (u32)(32 * rg + 16 + 4 * gq) * 4u);
    const f32x4 vr0 = lds128f(L, L_RSTD + (u32)(32 * rg + 4 * gq) * 4u), vr1 = lds128f(L, L_RSTD + (u32)(32 * rg + 16 + 4 * gq) * 4u);
    u32x2 wp[2];
    auto logits = [&](int line, int pi, int rt) -> f32x4 {
      const int t = line * 2 + pi;
      const u32 ca = L_CSB + (u32)(line * 64 + (2 * cg + pi) * 16 + 4 * gq) * 8u;
      const f32x4 cb0 = lds128f(L, ca), cb1 = lds128f(L, ca + 16u);
      const float mn = rt ? mean1 : mean0, rs = rt ? rstd1 : rstd0;
      const float y0 = rs * fmaf(-mn, cb0[0], acc[t][rt][0]), y1 = rs * fmaf(-mn, cb0[2], acc[t][rt][1]);
      const float y2 = rs * fmaf(-mn, cb1[0], acc[t][rt][2]), y3 = rs * fmaf(-mn, cb1[2], acc[t][rt][3]);
      const u32x2 kb = {pk2(y0, y1), pk2(y2, y3)};
      return mfma16(wp[pi], kb, zero4);
    };
#pragma unroll
    for (int pi = 0; pi < 2; ++pi)
      wp[pi] = lds_tr(L, L_WPHI + (u32)((2 * cg + pi) * 512 + (4 * gq + q4) * 32 + pp * 8));
    f32x4 kvt[4], zt[4];
#pragma unroll
    for (int pi = 0; pi < 2; ++pi) {
      u32x4 tp, tn, vv;
#pragma unroll
      for (int rt = 0; rt < 2; ++rt) {
        const u32x4 ph = dual_softmax(logits(1, pi, rt));
        const f32x4 a = mfma16(u32x2{ph[0], ph[1]}, ident, zero4);
        const f32x4 n = mfma16(u32x2{ph[2], ph[3]}, ident, zero4);
        tp[2 * rt] = pk2(a[0], a[1]); tp[2 * rt + 1] = pk2(a[2], a[3]);
        tn[2 * rt] = pk2(n[0], n[1]); tn[2 * rt + 1] = pk2(n[2], n[3]);
        const float cs = *(AS3 float*)(L + L_CSB + (u32)(128 + (2 * cg + pi) * 16 + r16) * 8u);
        const f32x4 vm = rt ? vm1 : vm0, vr = rt ? vr1 : vr0;
        const f32x4 av = acc[4 + pi][rt];
        vv[2 * rt] = pk2(vr[0] * fmaf(-vm[0], cs, av[0]), vr[1] * fmaf(-vm[1], cs, av[1]));
        vv[2 * rt + 1] = pk2(vr[2] * fmaf(-vm[2], cs, av[2]), vr[3] * fmaf(-vm[3], cs, av[3]));
      }
      kvt[2 * pi] = mfma32(tp, vv, zero4); kvt[2 * pi + 1] = mfma32(tn, vv, zero4);
      zt[2 * pi] = mfma32(tp, ones, zero4); zt[2 * pi + 1] = mfma32(tn, ones, zero4);
    }
    {
      const int sl = rg > 0 ? rg - 1 : 0;
#pragma unroll
      for (int pi = 0; pi < 2; ++pi) {
        if (rg != pi) {
#pragma unroll
          for (int sg = 0; sg < 2; ++sg)
            *(AS3 f32x4*)(L + L_SCR + (u32)((((cg * 2 + pi) * 2 + sg) * 3 + sl) * 1024 + l * 16)) = kvt[2 * pi + sg];
        }
        if (r16 == 0) {
#pragma unroll
          for (int sg = 0; sg < 2; ++sg)
            *(AS3 f32x4*)(L + L_ZR + (u32)(((((cg * 2 + pi) * 2 + sg) * 4 + rg) * 16 + 4 * gq) * 4)) = zt[2 * pi + sg];
        }
      }
    }
    WAIT_L0();
    BAR();
    if (rg < 2) {
      f32x4 sp = rg == 0 ? kvt[0] : kvt[2], sn = rg == 0 ? kvt[1] : kvt[3];
#pragma unroll
      for (int sl = 0; sl < 3; ++sl) {
        sp += lds128f(L, L_SCR + (u32)((((cg * 2 + rg) * 2 + 0) * 3 + sl) * 1024 + l * 16));
        sn += lds128f(L, L_SCR + (u32)((((cg * 2 + rg) * 2 + 1) * 3 + sl) * 1024 + l * 16));
      }
      u32x4 o = {pk2(sp[0], sp[1]), pk2(sp[2], sp[3]), pk2(sn[0], sn[1]), pk2(sn[2], sn[3])};
      st_sc1_x4(ws, WS_KVP + (u32)(((g * 16 + p) * 4 + 2 * cg + rg) * 1024 + l * 16), o);
    } else if (l < 8) {
      const int pi = rg - 2, sg = l >> 2, qq = l & 3;
      f32x4 s = {0, 0, 0, 0};
#pragma unroll
      for (int r = 0; r < 4; ++r) s += lds128f(L, L_ZR + (u32)(((((cg * 2 + pi) * 2 + sg) * 4 + r) * 16 + 4 * qq) * 4));
      st_sc1_x4(ws, WS_ZP + (u32)((((g * 4 + 2 * cg + pi) * 16 + p) * 32 + l * 4) * 4), __builtin_bit_cast(u32x4, s));
    }
    WAIT_V(0);
    BAR();
    if (threadIdx.x == 0) st_sc1_x1(ws, WS_KVFLAG + (u32)g * 128u + (u32)p * 4u, tag);
    STAMP(4);
    loadBreg();
    u32x4 phq[2][2];
#pragma unroll
    for (int pi = 0; pi < 2; ++pi)
#pragma unroll
      for (int rt = 0; rt < 2; ++rt) phq[pi][rt] = dual_softmax(logits(0, pi, rt));
    STAMP(5);
    if (w == 0) poll_flags(ws, WS_KVFLAG + (u32)g * 128u, 16, l, tag);
    BAR();
    STAMP(6);
    {
      const int hd = w & 3, hf = w >> 2;
#pragma unroll
      for (int i = 0; i < 8; ++i) ld_sc1_x4(gat[i], ws, WS_KVP + (u32)(((g * 16 + 8 * hf + i) * 4 + hd) * 1024 + l * 16));
      u32x4 zg;
      ld_sc1_x4(zg, ws, WS_ZP + (u32)(((g * 4 + hd) * 16 + 8 * hf) * 128 + l * 16));
      asm volatile("s_waitcnt vmcnt(0)" : "+v"(gat[0]), "+v"(gat[1]), "+v"(gat[2]), "+v"(gat[3]), "+v"(gat[4]), "+v"(gat[5]),
                   "+v"(gat[6]), "+v"(gat[7]), "+v"(zg) :: "memory");
      f32x4 s0 = {0, 0, 0, 0}, s1 = {0, 0, 0, 0};
#pragma unroll
      for (int i = 0; i < 8; ++i) {
        s0[0] += bflo(gat[i][0]); s0[1] += bfhi(gat[i][0]); s0[2] += bflo(gat[i][1]); s0[3] += bfhi(gat[i][1]);
        s1[0] += bflo(gat[i][2]); s1[1] += bfhi(gat[i][2]); s1[2] += bflo(gat[i][3]); s1[3] += bfhi(gat[i][3]);
      }
      *(AS3 f32x4*)(L + L_SCR + (u32)((hd * 2 + hf) * 2048 + l * 32)) = s0;
      *(AS3 f32x4*)(L + L_SCR + (u32)((hd * 2 + hf) * 2048 + l * 32 + 16)) = s1;
      f32x4 zs = __builtin_bit_cast(f32x4, zg);
#pragma unroll
      for (int r = 0; r < 4; ++r) {
        float v = zs[r];
        v += __shfl_xor(v, 8); v += __shfl_xor(v, 16); v += __shfl_xor(v, 32);
        zs[r] = v;
      }
      if (l < 8) *(AS3 f32x4*)(L + L_ZR + (u32)(((hd * 2 + hf) * 32 + l * 4) * 4)) = zs;
    }
    WAIT_L0();
    BAR();
    {
      u32x4 o0, o1;
#pragma unroll
      for (int pi = 0; pi < 2; ++pi) {
        const int hd = 2 * cg + pi;
        const u32 ga = L_SCR + (u32)(hd * 4096 + l * 32);
        const f32x4 kp = lds128f(L, ga) + lds128f(L, ga + 2048u), kn = lds128f(L, ga + 16u) + lds128f(L, ga + 2064u);
        const u32 za = L_ZR + (u32)((hd * 64 + 4 * gq) * 4);
        const f32x4 zp = lds128f(L, za) + lds128f(L, za + 128u), zn = lds128f(L, za + 64u) + lds128f(L, za + 192u);
        const u32x4 kf = {pk2(kp[0], kp[1]), pk2(kp[2], kp[3]), pk2(kn[0], kn[1]), pk2(kn[2], kn[3])};
        const u32x4 zf = {pk2(zp[0], zp[1]), pk2(zp[2], zp[3]), pk2(zn[0], zn[1]), pk2(zn[2], zn[3])};
#pragma unroll
        for (int rt = 0; rt < 2; ++rt) {
          const f32x4 num = mfma32(kf, phq[pi][rt], zero4), den = mfma32(zf, phq[pi][rt], zero4);
          const float rd = __builtin_amdgcn_rcpf(den[0] + 1e-6f);
          const u32 a = pk2(num[0] * rd, num[1] * rd), bq = pk2(num[2] * rd, num[3] * rd);
          if (rt == 0) { o0[2 * pi] = a; o0[2 * pi + 1] = bq; } else { o1[2 * pi] = a; o1[2 * pi + 1] = bq; }
        }
      }
      const u32 off = WS_AF + ((u32)(8 * p + 2 * rg) * 72u + (u32)(56 + 2 * g + cg)) * 1024u + (u32)l * 16u;
      st_sc1_x4(ws, off, o0);
      st_sc1_x4(ws, off + 72u * 1024u, o1);
    }
    WAIT_V(0);
    BAR();
    if (threadIdx.x == 0) st_sc1_x1(ws, WS_OFLAG + (u32)p * 128u + (u32)g * 4u, tag);
    STAMP(7);
  }

  {
    const int dp = rb >> 1;
    u32x4 xres, b2v;
    const u32 fo = (u32)(64 * rb + (threadIdx.x >> 3)) * 1024u + (u32)(cb * 128 + (threadIdx.x & 7) * 16);
    if (w == 0) poll_flags(ws, WS_HFLAG + (u32)dp * 128u, 8, l, tag);
    BAR();
    STAMP(8);
    const char* af = ws + WS_AF + (size_t)(4 * rb) * 73728u;
    const char* afh = af + (size_t)(7 * w) * 1024u;
    const char* afo = af + (size_t)(56 + 2 * w) * 1024u;
    const u32 vo = (u32)l * 16u;
    f32x4 dacc[4][4];
#pragma unroll
    for (int i = 0; i < 4; ++i)
#pragma unroll
      for (int j = 0; j < 4; ++j) dacc[i][j] = f32x4{0, 0, 0, 0};
    u32x4 a[9][4];
    auto loadA = [&](int j) {
      const char* s = j < 7 ? afh + j * 1024 : afo + (j - 7) * 1024;
#pragma unroll
      for (int rt = 0; rt < 4; ++rt) ld_sc1_x4(a[j][rt], s + rt * 73728, vo);
    };
    auto waitA = [&](int j, auto n) { wait4<decltype(n)::value>(a[j][0], a[j][1], a[j][2], a[j][3]); };
    auto step = [&](int j) {
#pragma unroll
      for (int ct = 0; ct < 4; ++ct) {
        const u32x4 bf = cat(blo[j & 3][ct], bhi[j & 3][ct]);
#pragma unroll
        for (int rt = 0; rt < 4; ++rt) dacc[rt][ct] = mfma32(bf, a[j][rt], dacc[rt][ct]);
      }
      SCHED();
    };
    loadA(0); loadA(1); loadA(2); loadA(3);
    ld_x4(xres, x, fo);
    waitA(0, IC<13>{}); step(0); rdB(IC<4>{}); loadA(4);
    waitA(1, IC<13>{}); waitb(blo[0], bhi[0]); issueB(8); step(1); rdB(IC<5>{}); loadA(5);
    waitA(2, IC<17>{}); step(2); rdB(IC<6>{}); loadA(6);
    waitA(3, IC<17>{}); step(3); rdB(IC<7>{});
    waitA(4, IC<12>{}); step(4);
    waitA(5, IC<4>{}); waitb(blo[1], bhi[1]); step(5); rdB(IC<8>{});
    waitA(6, IC<0>{}); waitb(blo[2], bhi[2]); step(6);
    STAMP(9);
    poll_flags(ws, WS_OFLAG + (u32)dp * 128u + (u32)w * 4u, 1, l, tag);
    STAMP(10);
    loadA(7); loadA(8);
    ld_x4(b2v, b2, (u32)(cb * 128 + (threadIdx.x & 7) * 16));
    waitA(7, IC<5>{}); waitb(blo[3], bhi[3]); step(7);
    waitA(8, IC<1>{}); waitb(blo[0], bhi[0]); step(8);
    WAIT_L0();
#pragma unroll
    for (int rt = 0; rt < 4; ++rt)
#pragma unroll
      for (int ct = 0; ct < 4; ++ct)
        *(AS3 f32x4*)(L + dslot(rt) + (u32)(r16 * 256 + (((ct * 4 + gq) ^ r16) * 16))) = dacc[rt][ct];
    WAIT_L0();
    BAR();
    STAMP(11);
    {
      const int row = threadIdx.x >> 3, cq = threadIdx.x & 7, rt = row >> 4, tk = row & 15;
      const u32 pb = (u32)((rt + 3) & 3) * 32768u + (u32)(tk * 256);
      f32x4 s0 = {0, 0, 0, 0}, s1 = {0, 0, 0, 0};
#pragma unroll
      for (int ww = 0; ww < 8; ++ww) {
        s0 += lds128f(L, pb + (u32)(ww * 4096) + (u32)(((2 * cq) ^ tk) * 16));
        s1 += lds128f(L, pb + (u32)(ww * 4096) + (u32)(((2 * cq + 1) ^ tk) * 16));
      }
      asm volatile("s_waitcnt vmcnt(0)" : "+v"(xres), "+v"(b2v) :: "memory");
      u32x4 o;
      o[0] = pk2(s0[0] + bflo(xres[0]) + bflo(b2v[0]), s0[1] + bfhi(xres[0]) + bfhi(b2v[0]));
      o[1] = pk2(s0[2] + bflo(xres[1]) + bflo(b2v[1]), s0[3] + bfhi(xres[1]) + bfhi(b2v[1]));
      o[2] = pk2(s1[0] + bflo(xres[2]) + bflo(b2v[2]), s1[1] + bfhi(xres[2]) + bfhi(b2v[2]));
      o[3] = pk2(s1[2] + bflo(xres[3]) + bflo(b2v[3]), s1[3] + bfhi(xres[3]) + bfhi(b2v[3]));
      *(u32x4*)(out + fo) = o;
    }
    if (b == 255 && w == 7) {
#pragma unroll 1
      for (int i = 0; i < 4; ++i) poll_flags(ws, WS_AFLAG + (u32)i * 256u, 64, l, tag);
      if (l == 0) st_sc1_x1(ws, WS_EPOCH, tag);
    }
    STAMP(12);
    STAMP_FLUSH;
  }
}

__global__ void report(const char* ws) {
  const u64* tm = (const u64*)(ws + WS_TIM);
  const int l = threadIdx.x;
  u64 now; asm volatile("s_memrealtime %0\n\ts_waitcnt lgkmcnt(0)" : "=s"(now) :: "memory");
  __shared__ u64 t0s; __shared__ float res[2][13][3];
  u64 t0 = ~0ull;
  for (int i = l; i < 2048; i += 64) { u64 t = tm[i * 32]; t0 = t < t0 ? t : t0; }
  for (int o = 32; o; o >>= 1) { u64 v = __shfl_xor(t0, o); t0 = v < t0 ? v : t0; }
  for (int ty = 0; ty < 2; ++ty)
    for (int s = 0; s < 13; ++s) {
      float mn = 1e9f, mx = 0.f, av = 0.f; int n = 0;
      for (int i = l; i < 2048; i += 64) { int b = i >> 3; if (((b >> 3) & 1) != ty) continue; u64 t = tm[i * 32 + s]; if (t < t0 || t - t0 > 100000) continue;
        float v = (float)(t - t0) * 0.01f; mn = fminf(mn, v); mx = fmaxf(mx, v); av += v; ++n; }
      for (int o = 32; o; o >>= 1) { mn = fminf(mn, __shfl_xor(mn, o)); mx = fmaxf(mx, __shfl_xor(mx, o)); av += __shfl_xor(av, o); n += __shfl_xor(n, o); }
      if (l == 0) { res[ty][s][0] = mn; res[ty][s][1] = n ? av / n : 0.f; res[ty][s][2] = mx; }
    }
  __syncthreads();
  if (l == 0) {
    printf("TS rep_start %.2f | A avg(max): 0:%.2f(%.2f) 1:%.2f(%.2f) 2:%.2f(%.2f) 3:%.2f(%.2f) 4:%.2f(%.2f) 5:%.2f(%.2f) 6:%.2f(%.2f) 7:%.2f(%.2f) 8:%.2f(%.2f) 9:%.2f(%.2f) 10:%.2f(%.2f) 11:%.2f(%.2f) 12:%.2f(%.2f) | B: 0:%.2f(%.2f) 1:%.2f(%.2f) 2:%.2f(%.2f) 3:%.2f(%.2f) 4:%.2f(%.2f) 8:%.2f(%.2f) 9:%.2f(%.2f) 10:%.2f(%.2f) 11:%.2f(%.2f) 12:%.2f(%.2f)\n",
      (float)(now - t0) * 0.01f,
      res[0][0][1], res[0][0][2], res[0][1][1], res[0][1][2], res[0][2][1], res[0][2][2], res[0][3][1], res[0][3][2], res[0][4][1], res[0][4][2], res[0][5][1], res[0][5][2],
      res[0][6][1], res[0][6][2], res[0][7][1], res[0][7][2], res[0][8][1], res[0][8][2], res[0][9][1], res[0][9][2], res[0][10][1], res[0][10][2], res[0][11][1], res[0][11][2], res[0][12][1], res[0][12][2],
      res[1][0][1], res[1][0][2], res[1][1][1], res[1][1][2], res[1][2][1], res[1][2][2], res[1][3][1], res[1][3][2], res[1][4][1], res[1][4][2],
      res[1][8][1], res[1][8][2], res[1][9][1], res[1][9][2], res[1][10][1], res[1][10][2], res[1][11][1], res[1][11][2], res[1][12][1], res[1][12][2]);
  }
}
extern "C" void kernel_launch(void* const* d_in, const int* in_sizes, int n_in, void* d_out, int out_size, void* d_ws,
                              size_t ws_size, hipStream_t stream) {
  (void)in_sizes; (void)n_in; (void)out_size; (void)ws_size;
  hipFuncSetAttribute((const void*)fused_kernel, hipFuncAttributeMaxDynamicSharedMemorySize, LDS_BYTES);
  fused_kernel<<<256, 512, LDS_BYTES, stream>>>((const char*)d_in[0], (const char*)d_in[1], (const char*)d_in[2],
                                                (const char*)d_in[3], (const char*)d_in[5], (const char*)d_in[6],
                                                (const char*)d_in[7], (const char*)d_in[8], (char*)d_out, (char*)d_ws);
  report<<<1, 64, 0, stream>>>((const char*)d_ws);
}
```

```cpp
#include <hip/hip_runtime.h>
#include <stdint.h>

#define TIMING 1
#define RTYPE 2
#define RSTAMP 0
#define RSTAT 0
#define EXP_GL 1
typedef unsigned int u32;
typedef unsigned long long u64;
typedef __bf16 bf16x8 __attribute__((ext_vector_type(8)));
typedef __bf16 bf16x2 __attribute__((ext_vector_type(2)));
typedef short v4i16 __attribute__((ext_vector_type(4)));
typedef float f32x4 __attribute__((ext_vector_type(4)));
typedef float f32x2 __attribute__((ext_vector_type(2)));
typedef u32 u32x4 __attribute__((ext_vector_type(4)));
typedef u32 u32x2 __attribute__((ext_vector_type(2)));

#define AS1 __attribute__((address_space(1)))
#define AS3 __attribute__((address_space(3)))
#define AS4 __attribute__((address_space(4)))
#define DEVI __device__ __forceinline__

constexpr u32 WS_EPOCH = 0;
constexpr u32 WS_AFLAG = 1024;
constexpr u32 WS_HFLAG = 2048;
constexpr u32 WS_OFLAG = 4096;
constexpr u32 WS_KVFLAG = 6144;
constexpr u32 WS_STAT = 8192;
constexpr u32 WS_CS = 40960;
constexpr u32 WS_KVP = 0x50000;
constexpr u32 WS_ZP = 0xD0000;
constexpr u32 WS_AF = 0x100000;
constexpr u32 WS_TIM = 0xB00000;
constexpr u32 WS_USED = 0xC00000;

constexpr u32 SLOT = 49152;
constexpr u32 L_SCR = 131072;
constexpr u32 L_ZR = L_SCR + 24576;
constexpr u32 L_CSB = 157696;
constexpr u32 L_MEAN = 159744;
constexpr u32 L_RSTD = 160256;
constexpr u32 L_WPHI = 160768;
constexpr u32 LDS_BYTES = 162816;

DEVI f32x4 mfma32(u32x4 a, u32x4 b, f32x4 c) {
  return __builtin_amdgcn_mfma_f32_16x16x32_bf16(__builtin_bit_cast(bf16x8, a), __builtin_bit_cast(bf16x8, b), c, 0, 0, 0);
}
DEVI f32x4 mfma16(u32x2 a, u32x2 b, f32x4 c) {
  return __builtin_amdgcn_mfma_f32_16x16x16bf16_1k(__builtin_bit_cast(v4i16, a), __builtin_bit_cast(v4i16, b), c, 0, 0, 0);
}
DEVI u32 pk2(float a, float b) {
  f32x2 v = {a, b};
  return __builtin_bit_cast(u32, __builtin_convertvector(v, bf16x2));
}
DEVI float bflo(u32 v) { return __builtin_bit_cast(float, v << 16); }
DEVI float bfhi(u32 v) { return __builtin_bit_cast(float, v & 0xffff0000u); }
DEVI u32 fbits(float f) { return __builtin_bit_cast(u32, f); }
DEVI float bitsf(u32 u) { return __builtin_bit_cast(float, u); }

DEVI u32x2 lds_tr(AS3 char* L, u32 addr) {
  v4i16 t = __builtin_amdgcn_ds_read_tr16_b64_v4i16((AS3 v4i16*)(L + addr));
  return __builtin_bit_cast(u32x2, t);
}
DEVI u32x4 lds_frag_tr(AS3 char* L, u32 addr, u32 second) {
  u32x2 a = lds_tr(L, addr), b = lds_tr(L, addr + second);
  u32x4 r = {a[0], a[1], b[0], b[1]};
  return r;
}
DEVI u32x4 lds128(AS3 char* L, u32 addr) { return *(AS3 u32x4*)(L + addr); }
DEVI f32x4 lds128f(AS3 char* L, u32 addr) { return *(AS3 f32x4*)(L + addr); }
template <int OFF> DEVI void glds_m0(u32 m0v, const char* sbase, u32 voff) {
  asm volatile("s_mov_b32 m0, %0\n\ts_nop 0\n\tglobal_load_lds_dwordx4 %1, %2 offset:%3" ::"s"(m0v), "v"(voff), "s"(sbase), "n"(OFF) : "memory");
}
template <int OFF> DEVI void glds(const char* sbase, u32 voff) {
  asm volatile("global_load_lds_dwordx4 %0, %1 offset:%2" ::"v"(voff), "s"(sbase), "n"(OFF) : "memory");
}
template <int OFF> DEVI void glds_masked(const char* sbase, u32 voff, u64 mask) {
  asm volatile("s_mov_b64 exec, %3\n\tglobal_load_lds_dwordx4 %0, %1 offset:%2\n\ts_mov_b64 exec, -1" ::"v"(voff), "s"(sbase), "n"(OFF), "s"(mask) : "memory");
}
template <int OFF> DEVI void dsr128(u32x4& d, u32 a) {
  asm volatile("ds_read_b128 %0, %1 offset:%2" : "=v"(d) : "v"(a), "n"(OFF) : "memory");
}
template <int OFF> DEVI void dsrtr(u32x2& d, u32 a) {
  asm volatile("ds_read_b64_tr_b16 %0, %1 offset:%2" : "=v"(d) : "v"(a), "n"(OFF) : "memory");
}
DEVI u32x4 cat(u32x2 a, u32x2 b) { u32x4 r = {a[0], a[1], b[0], b[1]}; return r; }
#define WAIT_V(n) asm volatile("s_waitcnt vmcnt(%0)" ::"n"(n) : "memory")
#define WAIT_L0() asm volatile("s_waitcnt lgkmcnt(0)" ::: "memory")
#define BAR() __builtin_amdgcn_s_barrier()
#define SCHED() __builtin_amdgcn_sched_barrier(0)

DEVI void ld_sc1_x4(u32x4& d, const char* base, u32 off) {
  asm volatile("global_load_dwordx4 %0, %1, %2 sc1" : "=v"(d) : "v"(off), "s"(base) : "memory");
}
DEVI void ld_x4(u32x4& d, const char* base, u32 off) {
  asm volatile("global_load_dwordx4 %0, %1, %2" : "=v"(d) : "v"(off), "s"(base) : "memory");
}
DEVI void ld_x1(u32& d, const char* base, u32 off) {
  asm volatile("global_load_dword %0, %1, %2" : "=v"(d) : "v"(off), "s"(base) : "memory");
}
DEVI void ld_u16(u32& d, const char* base, u32 off) {
  asm volatile("global_load_ushort %0, %1, %2" : "=v"(d) : "v"(off), "s"(base) : "memory");
}
DEVI u32 ld_sc1_x1_wait(const char* base, u32 off) {
  u32 d;
  asm volatile("global_load_dword %0, %1, %2 sc1\n\ts_waitcnt vmcnt(0)" : "=v"(d) : "v"(off), "s"(base) : "memory");
  return d;
}
DEVI void st_sc1_x4(char* base, u32 off, u32x4 v) {
  asm volatile("global_store_dwordx4 %0, %1, %2 sc1\n\ts_nop 1" ::"v"(off), "v"(v), "s"(base) : "memory");
}
DEVI void st_sc1_x2(char* base, u32 off, u32x2 v) {
  asm volatile("global_store_dwordx2 %0, %1, %2 sc1" ::"v"(off), "v"(v), "s"(base) : "memory");
}
DEVI void st_sc1_x1(char* base, u32 off, u32 v) {
  asm volatile("global_store_dword %0, %1, %2 sc1" ::"v"(off), "v"(v), "s"(base) : "memory");
}
constexpr int SPIN_MAX = 1 << 17;
DEVI void poll_flags(const char* base, u32 off0, int n, int l, u32 tag) {
  u32 off = off0 + (u32)(l < n ? l : 0) * 4u;
#pragma unroll 1
  for (int it = 0; it < SPIN_MAX; ++it) {
    u32 v = ld_sc1_x1_wait(base, off);
    if (__builtin_amdgcn_ballot_w64(v != tag) == 0ull) break;
    __builtin_amdgcn_s_sleep(1);
  }
  __builtin_amdgcn_fence(__ATOMIC_ACQUIRE, "wavefront");
}

#define STAMPX(i) asm volatile("s_memrealtime %0\n\ts_waitcnt lgkmcnt(0)" : "=s"(ts[i]) :: "memory")
#define STAMP(i) STAMPX(i)
#define STAMPG(i) do {} while (0)
#define STAMP_DECL u64 ts[16] = {0, 0, 0, 0, 0, 0, 0, 0, 0, 0, 0, 0, 0, 0, 0, 0}; u64 tc0, tc1; \
  asm volatile("s_memtime %0\n\ts_waitcnt lgkmcnt(0)" : "=s"(tc0) :: "memory")
#define STAMP_FLUSH do { asm volatile("s_memtime %0\n\ts_waitcnt lgkmcnt(0)" : "=s"(tc1) :: "memory"); ts[15] = tc1 - tc0; \
  if (l == 0) { _Pragma("unroll") for (int _i = 0; _i < 16; ++_i) *(u64*)(ws + WS_TIM + ((b * 8 + w) * 32 + _i) * 8) = ts[_i]; } } while (0)

DEVI void red4_max2(float& a, float& b) {
  auto s = __builtin_amdgcn_permlane32_swap(fbits(a), fbits(b), false, false);
  float t = fmaxf(bitsf(s[0]), bitsf(s[1]));
  auto u = __builtin_amdgcn_permlane16_swap(fbits(t), fbits(t), false, false);
  float m = fmaxf(bitsf(u[0]), bitsf(u[1]));
  auto z = __builtin_amdgcn_permlane32_swap(fbits(m), fbits(m), false, false);
  a = bitsf(z[0]); b = bitsf(z[1]);
}
DEVI void red4_sum2_rcp(float& a, float& b) {
  auto s = __builtin_amdgcn_permlane32_swap(fbits(a), fbits(b), false, false);
  float t = bitsf(s[0]) + bitsf(s[1]);
  auto u = __builtin_amdgcn_permlane16_swap(fbits(t), fbits(t), false, false);
  float m = __builtin_amdgcn_rcpf(bitsf(u[0]) + bitsf(u[1]));
  auto z = __builtin_amdgcn_permlane32_swap(fbits(m), fbits(m), false, false);
  a = bitsf(z[0]); b = bitsf(z[1]);
}
DEVI u32x4 dual_softmax(f32x4 p) {
  const float L2E = 1.4426950408889634f;
  float mx = fmaxf(fmaxf(p[0], p[1]), fmaxf(p[2], p[3]));
  float nm = fmaxf(fmaxf(-p[0], -p[1]), fmaxf(-p[2], -p[3]));
  red4_max2(mx, nm);
  float a = -mx * L2E, c = -nm * L2E;
  float e0 = __builtin_amdgcn_exp2f(fmaf(p[0], L2E, a)), e1 = __builtin_amdgcn_exp2f(fmaf(p[1], L2E, a));
  float e2 = __builtin_amdgcn_exp2f(fmaf(p[2], L2E, a)), e3 = __builtin_amdgcn_exp2f(fmaf(p[3], L2E, a));
  float n0 = __builtin_amdgcn_exp2f(fmaf(p[0], -L2E, c)), n1 = __builtin_amdgcn_exp2f(fmaf(p[1], -L2E, c));
  float n2 = __builtin_amdgcn_exp2f(fmaf(p[2], -L2E, c)), n3 = __builtin_amdgcn_exp2f(fmaf(p[3], -L2E, c));
  float sp = (e0 + e1) + (e2 + e3), sn = (n0 + n1) + (n2 + n3);
  red4_sum2_rcp(sp, sn);
  u32x4 r = {pk2(e0 * sp, e1 * sp), pk2(e2 * sp, e3 * sp), pk2(n0 * sn, n1 * sn), pk2(n2 * sn, n3 * sn)};
  return r;
}

template <int V> struct IC { static constexpr int value = V; };
template <int N> DEVI void wait4(u32x4& a, u32x4& b, u32x4& c, u32x4& d) {
  asm volatile("s_waitcnt vmcnt(%4)" : "+v"(a), "+v"(b), "+v"(c), "+v"(d) : "n"(N) : "memory");
}
template <int N> DEVI void wait1(u32x4& a) { asm volatile("s_waitcnt vmcnt(%1)" : "+v"(a) : "n"(N) : "memory"); }

template <int N> DEVI void waitset(u32x4& a, u32x4& b, u32x2 (&lo)[8], u32x2 (&hi)[8]);
template <> DEVI void waitset<8>(u32x4& a, u32x4& b, u32x2 (&lo)[8], u32x2 (&hi)[8]) {
  asm volatile("s_waitcnt lgkmcnt(0)"
               : "+v"(a), "+v"(b), "+v"(lo[0]), "+v"(lo[1]), "+v"(lo[2]), "+v"(lo[3]), "+v"(lo[4]), "+v"(lo[5]), "+v"(lo[6]), "+v"(lo[7]),
                 "+v"(hi[0]), "+v"(hi[1]), "+v"(hi[2]), "+v"(hi[3]), "+v"(hi[4]), "+v"(hi[5]), "+v"(hi[6]), "+v"(hi[7])
               :: "memory");
}
template <> DEVI void waitset<6>(u32x4& a, u32x4& b, u32x2 (&lo)[8], u32x2 (&hi)[8]) {
  asm volatile("s_waitcnt lgkmcnt(0)"
               : "+v"(a), "+v"(b), "+v"(lo[0]), "+v"(lo[1]), "+v"(lo[2]), "+v"(lo[3]), "+v"(lo[4]), "+v"(lo[5]),
                 "+v"(hi[0]), "+v"(hi[1]), "+v"(hi[2]), "+v"(hi[3]), "+v"(hi[4]), "+v"(hi[5])
               :: "memory");
}
DEVI void waitb(u32x2 (&lo)[4], u32x2 (&hi)[4]) {
  asm volatile("s_waitcnt lgkmcnt(0)"
               : "+v"(lo[0]), "+v"(lo[1]), "+v"(lo[2]), "+v"(lo[3]), "+v"(hi[0]), "+v"(hi[1]), "+v"(hi[2]), "+v"(hi[3]) :: "memory");
}
DEVI float wave_sum(float v) {
#define DPP_ADD(ctrl, rmask) v += bitsf((u32)__builtin_amdgcn_update_dpp(0, (int)fbits(v), ctrl, rmask, 0xf, false))
  DPP_ADD(0x111, 0xf); DPP_ADD(0x112, 0xf); DPP_ADD(0x114, 0xf); DPP_ADD(0x118, 0xf);
  DPP_ADD(0x142, 0xa); DPP_ADD(0x143, 0xc);
#undef DPP_ADD
  return bitsf((u32)__builtin_amdgcn_readlane((int)fbits(v), 63));
}

extern "C" __global__ void __launch_bounds__(512, 2)
fused_kernel(const char* __restrict__ x, const char* __restrict__ wqkv, const char* __restrict__ wphi,
             const char* __restrict__ wo, const char* __restrict__ w1, const char* __restrict__ b1,
             const char* __restrict__ w2, const char* __restrict__ b2, char* __restrict__ out, char* __restrict__ ws) {
  extern __shared__ __attribute__((aligned(16))) char smem[];
  AS3 char* L = (AS3 char*)smem;
  asm volatile("" ::"s"(x), "s"(wqkv), "s"(wphi), "s"(wo), "s"(w1), "s"(b1), "s"(w2), "s"(b2), "s"(out), "s"(ws));
  const int b = blockIdx.x;
  const int l = threadIdx.x & 63;
  const int w = __builtin_amdgcn_readfirstlane(threadIdx.x >> 6);
  const int xcd = b & 7, bi = b >> 3;
  const int p = (xcd >> 2) * 8 + (bi >> 2), c = (xcd & 3) * 4 + (bi & 3), g = c >> 1;
  const bool typeA = (c & 1) == 0;
  const int rg = w & 3, cg = w >> 2;
  const int gq = l >> 4, r16 = l & 15, q4 = (l >> 2) & 3, pp = l & 3;
  const int rot = (p + c) & 7;
  const int rb = 4 * xcd + (bi >> 3), cb = bi & 7;
  STAMP_DECL;
  STAMP(0);

  const u32 lb = (u32)(size_t)L;
  const u32 xrow_off = (u32)(128 * p + 8 * c + w) * 1024u + (u32)l * 16u;
  u32 gb1[2];
  const u32 gb1_off = (u32)(224 * g + (w < 6 ? 32 * w + ((g & 1) ? 32 : 0) : (w == 6 && !(g & 1) ? 192 : 0)) + (l >> 2)) * 2u;
  u32 tag;

  const char* xsrc = x + (size_t)p * 131072u;
  const u32 wstride = typeA ? 3072u : 3584u;
  const char* wsrc = (typeA ? (wqkv + g * 128) : (w1 + 448 * g)) - 4096;
  const u32 vx0 = (u32)(16 * w + (l >> 3)) * 1024u + (u32)(((l & 7) ^ (l >> 4)) * 16);
  const u32 vx1 = (u32)(16 * w + 7 + (l >> 3)) * 1024u + (u32)(((l & 7) ^ (4 + (l >> 4))) * 16);
  const u32 wsp = (u32)(((l & 7) >> 1) ^ (((l >> 4) & 1) | ((w & 1) << 1)));
  const u32 vw = (u32)(8 * w + (l >> 3)) * wstride + (wsp * 2u + (u32)(l & 1)) * 16u + 4096u;
  u32 vws[4];
  {
    const u32 odd = (u32)(g & 1) * 64u;
    vws[0] = typeA ? vw : vw + odd;
    vws[1] = typeA ? vw : vw + odd + 128u - 1024u;
    vws[2] = typeA ? vw : vw + odd + 256u - 2048u;
    vws[3] = vw + (odd ? 0u : 384u) - 3072u;
  }
  const u64 hmask = __builtin_amdgcn_ballot_w64(wsp < 2u);
  const u32 m0x = lb + (u32)w * 2048u, m0w = lb + 16384u + (u32)w * 4096u;

  const char* dsrc2 = w2 + (size_t)(7 * w) * 32768u + cb * 128;
  const char* dsrco = wo + (size_t)(2 * w) * 32768u + cb * 128;
  const u32 vdb = (u32)(l >> 3) * 1024u + (u32)(((((l & 7) >> 1) ^ ((l >> 4) & 3)) * 2 + (l & 1)) * 16);
  auto dslot = [&](int j) -> u32 { return (u32)((j + 3) & 3) * 32768u + (u32)w * 4096u; };
  auto issueB = [&](int j) {
    const char* s = (j < 7) ? (dsrc2 + j * 32768) : (dsrco + (j - 7) * 32768);
    glds_m0<0>(lb + dslot(j), s, vdb);
    glds<1024>(s, vdb + 7168u);
    glds<2048>(s, vdb + 2u * 7168u);
    glds<3072>(s, vdb + 3u * 7168u);
  };

  u32 xa0 = lb + (u32)(32 * rg + r16) * 128u + (u32)((gq ^ ((r16 >> 1) & 7)) * 16);
  u32 xa1 = xa0 ^ 64u;
  const u32 vbase = lb + 16384u + (u32)gq * 4096u + (u32)q4 * 128u + (u32)pp * 8u + (u32)(((q4 >> 1) & 1) | ((gq & 1) << 1)) * 32u;
  u32 R[8];
#pragma unroll
  for (int t = 0; t < 8; ++t) {
    const u32 ra = (vbase ^ (u32)((2 * cg + (t & 1)) * 32)) + (u32)(t >> 1) * 1024u;
    const u32 rbb = (vbase ^ (u32)((t & 3) * 32)) + (u32)(t >> 2) * 1024u + (u32)cg * 2048u;
    R[t] = typeA ? ra : rbb;
  }

  f32x4 acc[8][2];
#pragma unroll
  for (int t = 0; t < 8; ++t) { acc[t][0] = f32x4{0, 0, 0, 0}; acc[t][1] = f32x4{0, 0, 0, 0}; }
  u32x4 gat[8];
  const u32x4 ones = {0x3F803F80u, 0x3F803F80u, 0x3F803F80u, 0x3F803F80u};
  const u32 gcs = WS_CS + (u32)(c * 256 + 32 * w + (l >> 2)) * 64u + (u32)(l & 3) * 16u;
  const u32 gst = WS_STAT + (u32)(128 * p + 16 * w + r16) * 16u;

  auto g1 = [&](auto TAc, auto N8c) {
    constexpr bool TA = decltype(TAc)::value != 0, N8 = decltype(N8c)::value != 0;
    constexpr int NG = TA ? 5 : 6;
    u32x4 fx[2][2];
    u32x2 flo[2][8], fhi[2][8];
    u32x4 xrow;
    u32 epoch;
    asm volatile("s_load_dword %0, %1, 0x0" : "=s"(epoch) : "s"(ws) : "memory");
    ld_x4(xrow, x, xrow_off);
    if (TA) { gb1[0] = 0; gb1[1] = 0; }
    else { ld_u16(gb1[0], b1, gb1_off); ld_u16(gb1[1], b1, gb1_off + 32u); }
    auto issue = [&](int s, u32 sb) {
      const int ks = (s + rot) & 7;
      const char* xs = xsrc + ks * 128;
      const char* wsb = wsrc + (size_t)((u32)ks * 64u * wstride);
      glds_m0<0>(m0x + sb, xs, vx0);
      glds<1024>(xs, vx1);
      glds_m0<0>(m0w + sb, wsb, vws[0]);
      glds<1024>(wsb, vws[1]);
      glds<2048>(wsb, vws[2]);
      if (!TA) glds_masked<3072>(wsb, vws[3], hmask);
    };
    issue(0, 0); issue(1, SLOT); issue(2, 2 * SLOT);
    if (TA && w < 2) glds_m0<0>(lb + L_WPHI + (u32)w * 1024u, wphi + g * 2048 + w * 1024, (u32)l * 16u);

#define MF(S, t, rt) acc[t][rt] = mfma32(cat(flo[S][t], fhi[S][t]), fx[S][rt], acc[t][rt])
#define MFS(S, t, rt) acc[t][rt] = mfma32(fx[S][rt], cat(flo[S][t], fhi[S][t]), acc[t][rt])
#define RW(N, t) do { if (RD) { dsrtr<(N) * 16384>(flo[N][t], R[t]); dsrtr<(N) * 16384 + 512>(fhi[N][t], R[t]); } } while (0)
#define RX(N) do { if (RD) { dsr128<0>(fx[N][0], (N) ? xa1 : xa0); dsr128<2048>(fx[N][1], (N) ? xa1 : xa0); } } while (0)
    auto waitS = [&](auto Sc) {
      constexpr int S = decltype(Sc)::value;
      if constexpr (N8) waitset<8>(fx[S][0], fx[S][1], flo[S], fhi[S]);
      else waitset<6>(fx[S][0], fx[S][1], flo[S], fhi[S]);
    };
    auto phase = [&](auto Sc, auto RDc, auto GLc, const char* xs, const char* wsb, u32 sb) {
      constexpr int S = decltype(Sc)::value, N = 1 - S;
      constexpr bool RD = decltype(RDc)::value != 0, GL = decltype(GLc)::value != 0;
      waitS(Sc);
      if (S == 1 && RD) BAR();
      SCHED();
      if constexpr (N8) {
        MF(S, 6, 0); RW(N, 6); SCHED();
        MF(S, 6, 1); RW(N, 7); SCHED();
        MF(S, 7, 0); SCHED();
        MF(S, 7, 1); SCHED();
      }
      if constexpr (TA) {
        MFS(S, 4, 0); RX(N); SCHED();
        MFS(S, 4, 1); RW(N, 4); SCHED();
        MFS(S, 5, 0); RW(N, 5); SCHED();
        MFS(S, 5, 1); RW(N, 0); SCHED();
      } else {
        MF(S, 4, 0); RX(N); SCHED();
        MF(S, 4, 1); RW(N, 4); SCHED();
        MF(S, 5, 0); RW(N, 5); SCHED();
        MF(S, 5, 1); RW(N, 0); SCHED();
      }
      MF(S, 0, 0); RW(N, 1); SCHED();
      MF(S, 0, 1); RW(N, 2); SCHED();
      MF(S, 1, 0); RW(N, 3); SCHED();
      MF(S, 1, 1); if (GL) { glds_m0<0>(m0x + sb, xs, vx0); glds<1024>(xs, vx1); } SCHED();
      MF(S, 2, 0); if (GL) glds_m0<0>(m0w + sb, wsb, vws[0]); SCHED();
      MF(S, 2, 1); if (GL) glds<1024>(wsb, vws[1]); SCHED();
      MF(S, 3, 0); if (GL) glds<2048>(wsb, vws[2]); SCHED();
      MF(S, 3, 1); if (GL && !TA) glds_masked<3072>(wsb, vws[3], hmask); SCHED();
    };
    auto advance = [&](u32 d) {
#pragma unroll
      for (int t = 0; t < (N8 ? 8 : 6); ++t) R[t] += d;
      xa0 += d; xa1 += d;
    };

    STAMP(1);
    if (TA) asm volatile("s_waitcnt vmcnt(%1)" : "+v"(xrow) : "n"(2 * NG) : "memory");
    else asm volatile("s_waitcnt vmcnt(%3)" : "+v"(xrow), "+v"(gb1[0]), "+v"(gb1[1]) : "n"(2 * NG) : "memory");
    STAMPG(1);
    BAR();
    STAMPG(2);
    {
      constexpr bool RD = true;
      RX(0); RW(0, 4); RW(0, 5); RW(0, 0); RW(0, 1); RW(0, 2); RW(0, 3);
      if constexpr (N8) { RW(0, 6); RW(0, 7); }
    }
    waitS(IC<0>{});
    asm volatile("" : "+s"(epoch) :: "memory");
    tag = epoch + 1u;
    phase(IC<0>{}, IC<1>{}, IC<0>{}, nullptr, nullptr, 0u);
    {
      float v[8];
#pragma unroll
      for (int i = 0; i < 4; ++i) { v[2 * i] = bflo(xrow[i]); v[2 * i + 1] = bfhi(xrow[i]); }
      const float mean = wave_sum(((v[0] + v[1]) + (v[2] + v[3])) + ((v[4] + v[5]) + (v[6] + v[7]))) * (1.0f / 512.0f);
      float qq = 0.f;
#pragma unroll
      for (int i = 0; i < 8; ++i) { float d = v[i] - mean; qq = fmaf(d, d, qq); }
      const float rstd = 1.0f / sqrtf(wave_sum(qq) * (1.0f / 512.0f) + 1e-5f);
      if (l == 0) {
        u32x4 gr = {fbits(mean), fbits(rstd), tag, tag};
        st_sc1_x4(ws, WS_STAT + (u32)(128 * p + 8 * c + w) * 16u, gr);
        if (w == 7) st_sc1_x1(ws, WS_AFLAG + (u32)b * 4u, tag);
      }
      if (p < 8 && w < (TA ? 6 : 7)) {
        u32x2 c0l[2], c0h[2], c1l[2], c1h[2];
        const u32 a0 = (vbase ^ (u32)((2 * (w & 1)) * 32)) + (u32)(w >> 1) * 1024u;
        const u32 a1 = a0 ^ 32u;
        dsrtr<0>(c0l[0], a0); dsrtr<512>(c0h[0], a0); dsrtr<16384>(c0l[1], a0); dsrtr<16384 + 512>(c0h[1], a0);
        dsrtr<0>(c1l[0], a1); dsrtr<512>(c1h[0], a1); dsrtr<16384>(c1l[1], a1); dsrtr<16384 + 512>(c1h[1], a1);
        asm volatile("s_waitcnt lgkmcnt(0)" : "+v"(c0l[0]), "+v"(c0h[0]), "+v"(c0l[1]), "+v"(c0h[1]),
                     "+v"(c1l[0]), "+v"(c1h[0]), "+v"(c1l[1]), "+v"(c1h[1]) :: "memory");
        f32x4 c0 = {0, 0, 0, 0}, c1 = {0, 0, 0, 0};
#pragma unroll
        for (int ks = 0; ks < 2; ++ks) {
          c0 = mfma32(ones, cat(c0l[ks], c0h[ks]), c0);
          c1 = mfma32(ones, cat(c1l[ks], c1h[ks]), c1);
        }
        if (l < 32) {
          u32x2 gr = {fbits(l < 16 ? c0[0] : c1[0]), tag};
          st_sc1_x2(ws, WS_CS + (u32)((c * 256 + 32 * w + l) * 8 + rot) * 8u, gr);
        }
      }
    }
    u32 sb = 0;
#pragma unroll 1
    for (int s = 0; s < 5; ++s) {
      const u32 nsb = sb == 2 * SLOT ? 0u : sb + SLOT;
      advance(nsb - sb);
      const int ks = (s + 3 + rot) & 7;
      WAIT_V(NG);
      STAMPG(3 + s);
      phase(IC<1>{}, IC<1>{}, IC<EXP_GL>{}, xsrc + ks * 128, wsrc + (size_t)((u32)ks * 64u * wstride), sb);
      phase(IC<0>{}, IC<1>{}, IC<0>{}, nullptr, nullptr, 0u);
      sb = nsb;
    }
    advance(0u - 2u * SLOT);
    WAIT_V(NG);
    STAMPG(8);
    phase(IC<1>{}, IC<1>{}, IC<0>{}, nullptr, nullptr, 0u);
    ld_sc1_x4(gat[0], ws, gcs);
    ld_sc1_x4(gat[1], ws, gcs + 1024u);
    ld_sc1_x4(gat[2], ws, gst);
    issueB(0);
    phase(IC<0>{}, IC<1>{}, IC<0>{}, nullptr, nullptr, 0u);
    advance(SLOT);
    WAIT_V(7);
    STAMPG(9);
    phase(IC<1>{}, IC<1>{}, IC<0>{}, nullptr, nullptr, 0u);
    issueB(1);
    phase(IC<0>{}, IC<1>{}, IC<0>{}, nullptr, nullptr, 0u);
    phase(IC<1>{}, IC<0>{}, IC<0>{}, nullptr, nullptr, 0u);
#undef MF
#undef MFS
#undef RW
#undef RX
    STAMP(2);
    STAMPG(10);
    asm volatile("s_waitcnt vmcnt(8)" : "+v"(gat[0]), "+v"(gat[1]), "+v"(gat[2]) :: "memory");
    STAMPG(11);
  };
  if (typeA) g1(IC<1>{}, IC<0>{});
  else if (cg == 0) g1(IC<0>{}, IC<1>{});
  else g1(IC<0>{}, IC<0>{});
  {
    const bool used = w < (typeA ? 6 : 7);
  #pragma unroll 1
  for (int it = 0; it < SPIN_MAX; ++it) {
      const bool bad = (used && ((gat[0][1] != tag) | (gat[0][3] != tag) | (gat[1][1] != tag) | (gat[1][3] != tag))) | (gat[2][2] != tag);
      if (__builtin_amdgcn_ballot_w64(bad) == 0ull) break;
      ld_sc1_x4(gat[0], ws, gcs);
      ld_sc1_x4(gat[1], ws, gcs + 1024u);
      ld_sc1_x4(gat[2], ws, gst);
      asm volatile("s_waitcnt vmcnt(0)" : "+v"(gat[0]), "+v"(gat[1]), "+v"(gat[2]) :: "memory");
    }
#pragma unroll
    for (int i = 0; i < 2; ++i) {
      float sm = bitsf(gat[i][0]) + bitsf(gat[i][2]);
      sm += bitsf((u32)__builtin_amdgcn_update_dpp(0, (int)fbits(sm), 0xB1, 0xf, 0xf, false));
      sm += bitsf((u32)__builtin_amdgcn_update_dpp(0, (int)fbits(sm), 0x4E, 0xf, 0xf, false));
      if ((l & 3) == 0) {
        f32x2 o = {sm, bflo(gb1[i])};
        *(AS3 f32x2*)(L + L_CSB + (u32)(32 * w + 16 * i + (l >> 2)) * 8u) = o;
      }
    }
    if (l < 16) {
      *(AS3 float*)(L + L_MEAN + (u32)(16 * w + l) * 4u) = bitsf(gat[2][0]);
      *(AS3 float*)(L + L_RSTD + (u32)(16 * w + l) * 4u) = bitsf(gat[2][1]);
    }
  }
  WAIT_L0();
  BAR();
  issueB(2); issueB(3);
  STAMP(3);

  const float mean0 = *(AS3 float*)(L + L_MEAN + (u32)(32 * rg + r16) * 4u);
  const float mean1 = *(AS3 float*)(L + L_MEAN + (u32)(32 * rg + 16 + r16) * 4u);
  const float rstd0 = *(AS3 float*)(L + L_RSTD + (u32)(32 * rg + r16) * 4u);
  const float rstd1 = *(AS3 float*)(L + L_RSTD + (u32)(32 * rg + 16 + r16) * 4u);

  const u32 vb2 = (u32)(4 * gq + q4) * 128u + (u32)pp * 8u + (u32)((2 * gq + (q4 >> 1)) & 3) * 32u;
  u32x2 blo[4][4], bhi[4][4];
  u32 VB[4];
#pragma unroll
  for (int ct = 0; ct < 4; ++ct) VB[ct] = lb + (vb2 ^ (u32)(ct * 32)) + (u32)w * 4096u;
  auto rdB = [&](auto jc) {
    constexpr int j = decltype(jc)::value, blk = (j + 3) & 3;
#pragma unroll
    for (int ct = 0; ct < 4; ++ct) {
      const u32 a = VB[ct] + (blk >= 2 ? 65536u : 0u);
      dsrtr<(blk & 1) * 32768>(blo[j & 3][ct], a);
      dsrtr<(blk & 1) * 32768 + 2048>(bhi[j & 3][ct], a);
    }
  };
  auto loadBreg = [&]() {
    rdB(IC<0>{}); rdB(IC<1>{}); waitb(blo[0], bhi[0]); waitb(blo[1], bhi[1]);
    rdB(IC<2>{}); rdB(IC<3>{}); waitb(blo[2], bhi[2]); waitb(blo[3], bhi[3]);
    issueB(4); issueB(5); issueB(6); issueB(7);
  };

  if (!typeA) {
    auto hpair = [&](auto Pc) {
      constexpr int P = decltype(Pc)::value;
      const int hl = cg * 4 + P;
      u32x4 o0, o1;
#pragma unroll
      for (int e = 0; e < 2; ++e) {
        const int t = 2 * P + e;
        const u32 ca = L_CSB + (u32)(hl * 32 + e * 16 + 4 * gq) * 8u;
        const f32x4 cb0 = lds128f(L, ca), cb1 = lds128f(L, ca + 16u);
        const float cs[4] = {cb0[0], cb0[2], cb1[0], cb1[2]}, bb[4] = {cb0[1], cb0[3], cb1[1], cb1[3]};
        float h0[4], h1[4];
#pragma unroll
        for (int r = 0; r < 4; ++r) {
          float y0 = fmaf(rstd0, fmaf(-mean0, cs[r], acc[t][0][r]), bb[r]);
          float y1 = fmaf(rstd1, fmaf(-mean1, cs[r], acc[t][1][r]), bb[r]);
          y0 = fmaxf(y0, 0.f); y1 = fmaxf(y1, 0.f);
          h0[r] = y0 * y0; h1[r] = y1 * y1;
        }
        o0[2 * e] = pk2(h0[0], h0[1]); o0[2 * e + 1] = pk2(h0[2], h0[3]);
        o1[2 * e] = pk2(h1[0], h1[1]); o1[2 * e + 1] = pk2(h1[2], h1[3]);
      }
      const u32 step = (u32)(7 * g + ((g & 1) ? (hl < 6 ? hl + 1 : 0) : hl));
      const u32 off = WS_AF + ((u32)(8 * p + 2 * rg) * 72u + step) * 1024u + (u32)l * 16u;
      st_sc1_x4(ws, off, o0);
      st_sc1_x4(ws, off + 72u * 1024u, o1);
    };
    hpair(IC<0>{}); hpair(IC<1>{}); hpair(IC<2>{});
    if (cg == 0) hpair(IC<3>{});
    WAIT_V(0);
    BAR();
    if (threadIdx.x == 0) st_sc1_x1(ws, WS_HFLAG + (u32)p * 128u + (u32)g * 4u, tag);
    STAMP(4);
    loadBreg();
  } else {
    const u32x2 ident = {(4 * gq + 0 == r16 ? 0x3F80u : 0u) | (4 * gq + 1 == r16 ? 0x3F800000u : 0u),
                         (4 * gq + 2 == r16 ? 0x3F80u : 0u) | (4 * gq + 3 == r16 ? 0x3F800000u : 0u)};
    const f32x4 zero4 = {0, 0, 0, 0};
    const f32x4 vm0 = lds128f(L, L_MEAN + (u32)(32 * rg + 4 * gq) * 4u), vm1 = lds128f(L, L_MEAN + (u32)(32 * rg + 16 + 4 * gq) * 4u);
    const f32x4 vr0 = lds128f(L, L_RSTD + (u32)(32 * rg + 4 * gq) * 4u), vr1 = lds128f(L, L_RSTD + (u32)(32 * rg + 16 + 4 * gq) * 4u);
    u32x2 wp[2];
    auto logits = [&](int line, int pi, int rt) -> f32x4 {
      const int t = line * 2 + pi;
      const u32 ca = L_CSB + (u32)(line * 64 + (2 * cg + pi) * 16 + 4 * gq) * 8u;
      const f32x4 cb0 = lds128f(L, ca), cb1 = lds128f(L, ca + 16u);
      const float mn = rt ? mean1 : mean0, rs = rt ? rstd1 : rstd0;
      const float y0 = rs * fmaf(-mn, cb0[0], acc[t][rt][0]), y1 = rs * fmaf(-mn, cb0[2], acc[t][rt][1]);
      const float y2 = rs * fmaf(-mn, cb1[0], acc[t][rt][2]), y3 = rs * fmaf(-mn, cb1[2], acc[t][rt][3]);
      const u32x2 kb = {pk2(y0, y1), pk2(y2, y3)};
      return mfma16(wp[pi], kb, zero4);
    };
#pragma unroll
    for (int pi = 0; pi < 2; ++pi)
      wp[pi] = lds_tr(L, L_WPHI + (u32)((2 * cg + pi) * 512 + (4 * gq + q4) * 32 + pp * 8));
    f32x4 kvt[4], zt[4];
#pragma unroll
    for (int pi = 0; pi < 2; ++pi) {
      u32x4 tp, tn, vv;
#pragma unroll
      for (int rt = 0; rt < 2; ++rt) {
        const u32x4 ph = dual_softmax(logits(1, pi, rt));
        const f32x4 a = mfma16(u32x2{ph[0], ph[1]}, ident, zero4);
        const f32x4 n = mfma16(u32x2{ph[2], ph[3]}, ident, zero4);
        tp[2 * rt] = pk2(a[0], a[1]); tp[2 * rt + 1] = pk2(a[2], a[3]);
        tn[2 * rt] = pk2(n[0], n[1]); tn[2 * rt + 1] = pk2(n[2], n[3]);
        const float cs = *(AS3 float*)(L + L_CSB + (u32)(128 + (2 * cg + pi) * 16 + r16) * 8u);
        const f32x4 vm = rt ? vm1 : vm0, vr = rt ? vr1 : vr0;
        const f32x4 av = acc[4 + pi][rt];
        vv[2 * rt] = pk2(vr[0] * fmaf(-vm[0], cs, av[0]), vr[1] * fmaf(-vm[1], cs, av[1]));
        vv[2 * rt + 1] = pk2(vr[2] * fmaf(-vm[2], cs, av[2]), vr[3] * fmaf(-vm[3], cs, av[3]));
      }
      kvt[2 * pi] = mfma32(tp, vv, zero4); kvt[2 * pi + 1] = mfma32(tn, vv, zero4);
      zt[2 * pi] = mfma32(tp, ones, zero4); zt[2 * pi + 1] = mfma32(tn, ones, zero4);
    }
    {
      const int sl = rg > 0 ? rg - 1 : 0;
#pragma unroll
      for (int pi = 0; pi < 2; ++pi) {
        if (rg != pi) {
#pragma unroll
          for (int sg = 0; sg < 2; ++sg)
            *(AS3 f32x4*)(L + L_SCR + (u32)((((cg * 2 + pi) * 2 + sg) * 3 + sl) * 1024 + l * 16)) = kvt[2 * pi + sg];
        }
        if (r16 == 0) {
#pragma unroll
          for (int sg = 0; sg < 2; ++sg)
            *(AS3 f32x4*)(L + L_ZR + (u32)(((((cg * 2 + pi) * 2 + sg) * 4 + rg) * 16 + 4 * gq) * 4)) = zt[2 * pi + sg];
        }
      }
    }
    WAIT_L0();
    BAR();
    if (rg < 2) {
      f32x4 sp = rg == 0 ? kvt[0] : kvt[2], sn = rg == 0 ? kvt[1] : kvt[3];
#pragma unroll
      for (int sl = 0; sl < 3; ++sl) {
        sp += lds128f(L, L_SCR + (u32)((((cg * 2 + rg) * 2 + 0) * 3 + sl) * 1024 + l * 16));
        sn += lds128f(L, L_SCR + (u32)((((cg * 2 + rg) * 2 + 1) * 3 + sl) * 1024 + l * 16));
      }
      u32x4 o = {pk2(sp[0], sp[1]), pk2(sp[2], sp[3]), pk2(sn[0], sn[1]), pk2(sn[2], sn[3])};
      st_sc1_x4(ws, WS_KVP + (u32)(((g * 16 + p) * 4 + 2 * cg + rg) * 1024 + l * 16), o);
    } else if (l < 8) {
      const int pi = rg - 2, sg = l >> 2, qq = l & 3;
      f32x4 s = {0, 0, 0, 0};
#pragma unroll
      for (int r = 0; r < 4; ++r) s += lds128f(L, L_ZR + (u32)(((((cg * 2 + pi) * 2 + sg) * 4 + r) * 16 + 4 * qq) * 4));
      st_sc1_x4(ws, WS_ZP + (u32)((((g * 4 + 2 * cg + pi) * 16 + p) * 32 + l * 4) * 4), __builtin_bit_cast(u32x4, s));
    }
    WAIT_V(0);
    BAR();
    if (threadIdx.x == 0) st_sc1_x1(ws, WS_KVFLAG + (u32)g * 128u + (u32)p * 4u, tag);
    STAMP(4);
    loadBreg();
    u32x4 phq[2][2];
#pragma unroll
    for (int pi = 0; pi < 2; ++pi)
#pragma unroll
      for (int rt = 0; rt < 2; ++rt) phq[pi][rt] = dual_softmax(logits(0, pi, rt));
    STAMP(5);
    if (w == 0) poll_flags(ws, WS_KVFLAG + (u32)g * 128u, 16, l, tag);
    BAR();
    STAMP(6);
    {
      const int hd = w & 3, hf = w >> 2;
#pragma unroll
      for (int i = 0; i < 8; ++i) ld_sc1_x4(gat[i], ws, WS_KVP + (u32)(((g * 16 + 8 * hf + i) * 4 + hd) * 1024 + l * 16));
      u32x4 zg;
      ld_sc1_x4(zg, ws, WS_ZP + (u32)(((g * 4 + hd) * 16 + 8 * hf) * 128 + l * 16));
      asm volatile("s_waitcnt vmcnt(0)" : "+v"(gat[0]), "+v"(gat[1]), "+v"(gat[2]), "+v"(gat[3]), "+v"(gat[4]), "+v"(gat[5]),
                   "+v"(gat[6]), "+v"(gat[7]), "+v"(zg) :: "memory");
      f32x4 s0 = {0, 0, 0, 0}, s1 = {0, 0, 0, 0};
#pragma unroll
      for (int i = 0; i < 8; ++i) {
        s0[0] += bflo(gat[i][0]); s0[1] += bfhi(gat[i][0]); s0[2] += bflo(gat[i][1]); s0[3] += bfhi(gat[i][1]);
        s1[0] += bflo(gat[i][2]); s1[1] += bfhi(gat[i][2]); s1[2] += bflo(gat[i][3]); s1[3] += bfhi(gat[i][3]);
      }
      *(AS3 f32x4*)(L + L_SCR + (u32)((hd * 2 + hf) * 2048 + l * 32)) = s0;
      *(AS3 f32x4*)(L + L_SCR + (u32)((hd * 2 + hf) * 2048 + l * 32 + 16)) = s1;
      f32x4 zs = __builtin_bit_cast(f32x4, zg);
#pragma unroll
      for (int r = 0; r < 4; ++r) {
        float v = zs[r];
        v += __shfl_xor(v, 8); v += __shfl_xor(v, 16); v += __shfl_xor(v, 32);
        zs[r] = v;
      }
      if (l < 8) *(AS3 f32x4*)(L + L_ZR + (u32)(((hd * 2 + hf) * 32 + l * 4) * 4)) = zs;
    }
    WAIT_L0();
    BAR();
    {
      u32x4 o0, o1;
#pragma unroll
      for (int pi = 0; pi < 2; ++pi) {
        const int hd = 2 * cg + pi;
        const u32 ga = L_SCR + (u32)(hd * 4096 + l * 32);
        const f32x4 kp = lds128f(L, ga) + lds128f(L, ga + 2048u), kn = lds128f(L, ga + 16u) + lds128f(L, ga + 2064u);
        const u32 za = L_ZR + (u32)((hd * 64 + 4 * gq) * 4);
        const f32x4 zp = lds128f(L, za) + lds128f(L, za + 128u), zn = lds128f(L, za + 64u) + lds128f(L, za + 192u);
        const u32x4 kf = {pk2(kp[0], kp[1]), pk2(kp[2], kp[3]), pk2(kn[0], kn[1]), pk2(kn[2], kn[3])};
        const u32x4 zf = {pk2(zp[0], zp[1]), pk2(zp[2], zp[3]), pk2(zn[0], zn[1]), pk2(zn[2], zn[3])};
#pragma unroll
        for (int rt = 0; rt < 2; ++rt) {
          const f32x4 num = mfma32(kf, phq[pi][rt], zero4), den = mfma32(zf, phq[pi][rt], zero4);
          const float rd = __builtin_amdgcn_rcpf(den[0] + 1e-6f);
          const u32 a = pk2(num[0] * rd, num[1] * rd), bq = pk2(num[2] * rd, num[3] * rd);
          if (rt == 0) { o0[2 * pi] = a; o0[2 * pi + 1] = bq; } else { o1[2 * pi] = a; o1[2 * pi + 1] = bq; }
        }
      }
      const u32 off = WS_AF + ((u32)(8 * p + 2 * rg) * 72u + (u32)(56 + 2 * g + cg)) * 1024u + (u32)l * 16u;
      st_sc1_x4(ws, off, o0);
      st_sc1_x4(ws, off + 72u * 1024u, o1);
    }
    WAIT_V(0);
    BAR();
    if (threadIdx.x == 0) st_sc1_x1(ws, WS_OFLAG + (u32)p * 128u + (u32)g * 4u, tag);
    STAMP(7);
  }

  {
    const int dp = rb >> 1;
    u32x4 xres, b2v;
    const u32 fo = (u32)(64 * rb + (threadIdx.x >> 3)) * 1024u + (u32)(cb * 128 + (threadIdx.x & 7) * 16);
    if (w == 0) poll_flags(ws, WS_HFLAG + (u32)dp * 128u, 8, l, tag);
    BAR();
    STAMP(8);
    const char* af = ws + WS_AF + (size_t)(4 * rb) * 73728u;
    const char* afh = af + (size_t)(7 * w) * 1024u;
    const char* afo = af + (size_t)(56 + 2 * w) * 1024u;
    const u32 vo = (u32)l * 16u;
    f32x4 dacc[4][4];
#pragma unroll
    for (int i = 0; i < 4; ++i)
#pragma unroll
      for (int j = 0; j < 4; ++j) dacc[i][j] = f32x4{0, 0, 0, 0};
    u32x4 a[9][4];
    auto loadA = [&](int j) {
      const char* s = j < 7 ? afh + j * 1024 : afo + (j - 7) * 1024;
#pragma unroll
      for (int rt = 0; rt < 4; ++rt) ld_sc1_x4(a[j][rt], s + rt * 73728, vo);
    };
    auto waitA = [&](int j, auto n) { wait4<decltype(n)::value>(a[j][0], a[j][1], a[j][2], a[j][3]); };
    auto step = [&](int j) {
#pragma unroll
      for (int ct = 0; ct < 4; ++ct) {
        const u32x4 bf = cat(blo[j & 3][ct], bhi[j & 3][ct]);
#pragma unroll
        for (int rt = 0; rt < 4; ++rt) dacc[rt][ct] = mfma32(bf, a[j][rt], dacc[rt][ct]);
      }
      SCHED();
    };
    loadA(0); loadA(1); loadA(2); loadA(3);
    ld_x4(xres, x, fo);
    waitA(0, IC<13>{}); step(0); rdB(IC<4>{}); loadA(4);
    waitA(1, IC<13>{}); waitb(blo[0], bhi[0]); issueB(8); step(1); rdB(IC<5>{}); loadA(5);
    waitA(2, IC<17>{}); step(2); rdB(IC<6>{}); loadA(6);
    waitA(3, IC<17>{}); step(3); rdB(IC<7>{});
    waitA(4, IC<12>{}); step(4);
    waitA(5, IC<4>{}); waitb(blo[1], bhi[1]); step(5); rdB(IC<8>{});
    waitA(6, IC<0>{}); waitb(blo[2], bhi[2]); step(6);
    STAMP(9);
    poll_flags(ws, WS_OFLAG + (u32)dp * 128u + (u32)w * 4u, 1, l, tag);
    STAMP(10);
    loadA(7); loadA(8);
    ld_x4(b2v, b2, (u32)(cb * 128 + (threadIdx.x & 7) * 16));
    waitA(7, IC<5>{}); waitb(blo[3], bhi[3]); step(7);
    waitA(8, IC<1>{}); waitb(blo[0], bhi[0]); step(8);
    WAIT_L0();
#pragma unroll
    for (int rt = 0; rt < 4; ++rt)
#pragma unroll
      for (int ct = 0; ct < 4; ++ct)
        *(AS3 f32x4*)(L + dslot(rt) + (u32)(r16 * 256 + (((ct * 4 + gq) ^ r16) * 16))) = dacc[rt][ct];
    WAIT_L0();
    BAR();
    STAMP(11);
    {
      const int row = threadIdx.x >> 3, cq = threadIdx.x & 7, rt = row >> 4, tk = row & 15;
      const u32 pb = (u32)((rt + 3) & 3) * 32768u + (u32)(tk * 256);
      f32x4 s0 = {0, 0, 0, 0}, s1 = {0, 0, 0, 0};
#pragma unroll
      for (int ww = 0; ww < 8; ++ww) {
        s0 += lds128f(L, pb + (u32)(ww * 4096) + (u32)(((2 * cq) ^ tk) * 16));
        s1 += lds128f(L, pb + (u32)(ww * 4096) + (u32)(((2 * cq + 1) ^ tk) * 16));
      }
      asm volatile("s_waitcnt vmcnt(0)" : "+v"(xres), "+v"(b2v) :: "memory");
      u32x4 o;
      o[0] = pk2(s0[0] + bflo(xres[0]) + bflo(b2v[0]), s0[1] + bfhi(xres[0]) + bfhi(b2v[0]));
      o[1] = pk2(s0[2] + bflo(xres[1]) + bflo(b2v[1]), s0[3] + bfhi(xres[1]) + bfhi(b2v[1]));
      o[2] = pk2(s1[0] + bflo(xres[2]) + bflo(b2v[2]), s1[1] + bfhi(xres[2]) + bfhi(b2v[2]));
      o[3] = pk2(s1[2] + bflo(xres[3]) + bflo(b2v[3]), s1[3] + bfhi(xres[3]) + bfhi(b2v[3]));
      *(u32x4*)(out + fo) = o;
    }
    if (b == 255 && w == 7) {
#pragma unroll 1
      for (int i = 0; i < 4; ++i) poll_flags(ws, WS_AFLAG + (u32)i * 256u, 64, l, tag);
      if (l == 0) st_sc1_x1(ws, WS_EPOCH, tag);
    }
    STAMP(12);
    STAMP_FLUSH;
  }
}

__global__ void report(const char* ws) {
  const u64* tm = (const u64*)(ws + WS_TIM);
  const int l = threadIdx.x;
  u64 t0 = ~0ull;
  for (int i = l; i < 2048; i += 64) { u64 t = tm[i * 32]; t0 = t < t0 ? t : t0; }
  for (int o = 32; o; o >>= 1) { u64 v = __shfl_xor(t0, o); t0 = v < t0 ? v : t0; }
  float mn = 1e9f, mx = 0.f, av = 0.f; int n = 0;
  for (int i = l; i < 2048; i += 64) { int b = i >> 3; if (RTYPE < 2 && ((b >> 3) & 1) != RTYPE) continue; u64 t = tm[i * 32 + RSTAMP]; if (t < t0 || t - t0 > 100000) continue;
    float v = (float)(t - t0); mn = fminf(mn, v); mx = fmaxf(mx, v); av += v; ++n; }
  for (int o = 32; o; o >>= 1) { mn = fminf(mn, __shfl_xor(mn, o)); mx = fmaxf(mx, __shfl_xor(mx, o)); av += __shfl_xor(av, o); n += __shfl_xor(n, o); }
  float v = RSTAT == 0 ? 0.f : RSTAT == 1 ? av / (n ? n : 1) : RSTAT == 2 ? mx : mn;
  u64 now; asm volatile("s_memrealtime %0\n\ts_waitcnt lgkmcnt(0)" : "=s"(now) :: "memory");
  if (RSTAT == 4) v = (float)(now - t0);
  const u64 end = now + (u64)(v * 20.f);
  for (int it = 0; it < 2000000; ++it) {
    u64 t; asm volatile("s_memrealtime %0\n\ts_waitcnt lgkmcnt(0)" : "=s"(t) :: "memory");
    if (t >= end) break;
    __builtin_amdgcn_s_sleep(2);
  }
}
extern "C" void kernel_launch(void* const* d_in, const int* in_sizes, int n_in, void* d_out, int out_size, void* d_ws,
                              size_t ws_size, hipStream_t stream) {
  (void)in_sizes; (void)n_in; (void)out_size; (void)ws_size;
  hipFuncSetAttribute((const void*)fused_kernel, hipFuncAttributeMaxDynamicSharedMemorySize, LDS_BYTES);
  fused_kernel<<<256, 512, LDS_BYTES, stream>>>((const char*)d_in[0], (const char*)d_in[1], (const char*)d_in[2],
                                                (const char*)d_in[3], (const char*)d_in[5], (const char*)d_in[6],
                                                (const char*)d_in[7], (const char*)d_in[8], (char*)d_out, (char*)d_ws);
  report<<<1, 64, 0, stream>>>((const char*)d_ws);
}
```

```cpp
#include <hip/hip_runtime.h>
#include <stdint.h>

#define TIMING 1
#define MODE 5
#define EXP_GL 1
typedef unsigned int u32;
typedef unsigned long long u64;
typedef __bf16 bf16x8 __attribute__((ext_vector_type(8)));
typedef __bf16 bf16x2 __attribute__((ext_vector_type(2)));
typedef short v4i16 __attribute__((ext_vector_type(4)));
typedef float f32x4 __attribute__((ext_vector_type(4)));
typedef float f32x2 __attribute__((ext_vector_type(2)));
typedef u32 u32x4 __attribute__((ext_vector_type(4)));
typedef u32 u32x2 __attribute__((ext_vector_type(2)));

#define AS1 __attribute__((address_space(1)))
#define AS3 __attribute__((address_space(3)))
#define AS4 __attribute__((address_space(4)))
#define DEVI __device__ __forceinline__

constexpr u32 WS_EPOCH = 0;
constexpr u32 WS_AFLAG = 1024;
constexpr u32 WS_HFLAG = 2048;
constexpr u32 WS_OFLAG = 4096;
constexpr u32 WS_KVFLAG = 6144;
constexpr u32 WS_STAT = 8192;
constexpr u32 WS_CS = 40960;
constexpr u32 WS_KVP = 0x50000;
constexpr u32 WS_ZP = 0xD0000;
constexpr u32 WS_AF = 0x100000;
constexpr u32 WS_TIM = 0xB00000;
constexpr u32 WS_USED = 0xC00000;

constexpr u32 SLOT = 49152;
constexpr u32 L_SCR = 131072;
constexpr u32 L_ZR = L_SCR + 24576;
constexpr u32 L_CSB = 157696;
constexpr u32 L_MEAN = 159744;
constexpr u32 L_RSTD = 160256;
constexpr u32 L_WPHI = 160768;
constexpr u32 LDS_BYTES = 162816;

DEVI f32x4 mfma32(u32x4 a, u32x4 b, f32x4 c) {
  return __builtin_amdgcn_mfma_f32_16x16x32_bf16(__builtin_bit_cast(bf16x8, a), __builtin_bit_cast(bf16x8, b), c, 0, 0, 0);
}
DEVI f32x4 mfma16(u32x2 a, u32x2 b, f32x4 c) {
  return __builtin_amdgcn_mfma_f32_16x16x16bf16_1k(__builtin_bit_cast(v4i16, a), __builtin_bit_cast(v4i16, b), c, 0, 0, 0);
}
DEVI u32 pk2(float a, float b) {
  f32x2 v = {a, b};
  return __builtin_bit_cast(u32, __builtin_convertvector(v, bf16x2));
}
DEVI float bflo(u32 v) { return __builtin_bit_cast(float, v << 16); }
DEVI float bfhi(u32 v) { return __builtin_bit_cast(float, v & 0xffff0000u); }
DEVI u32 fbits(float f) { return __builtin_bit_cast(u32, f); }
DEVI float bitsf(u32 u) { return __builtin_bit_cast(float, u); }

DEVI u32x2 lds_tr(AS3 char* L, u32 addr) {
  v4i16 t = __builtin_amdgcn_ds_read_tr16_b64_v4i16((AS3 v4i16*)(L + addr));
  return __builtin_bit_cast(u32x2, t);
}
DEVI u32x4 lds_frag_tr(AS3 char* L, u32 addr, u32 second) {
  u32x2 a = lds_tr(L, addr), b = lds_tr(L, addr + second);
  u32x4 r = {a[0], a[1], b[0], b[1]};
  return r;
}
DEVI u32x4 lds128(AS3 char* L, u32 addr) { return *(AS3 u32x4*)(L + addr); }
DEVI f32x4 lds128f(AS3 char* L, u32 addr) { return *(AS3 f32x4*)(L + addr); }
template <int OFF> DEVI void glds_m0(u32 m0v, const char* sbase, u32 voff) {
  asm volatile("s_mov_b32 m0, %0\n\ts_nop 0\n\tglobal_load_lds_dwordx4 %1, %2 offset:%3" ::"s"(m0v), "v"(voff), "s"(sbase), "n"(OFF) : "memory");
}
template <int OFF> DEVI void glds(const char* sbase, u32 voff) {
  asm volatile("global_load_lds_dwordx4 %0, %1 offset:%2" ::"v"(voff), "s"(sbase), "n"(OFF) : "memory");
}
template <int OFF> DEVI void glds_masked(const char* sbase, u32 voff, u64 mask) {
  asm volatile("s_mov_b64 exec, %3\n\tglobal_load_lds_dwordx4 %0, %1 offset:%2\n\ts_mov_b64 exec, -1" ::"v"(voff), "s"(sbase), "n"(OFF), "s"(mask) : "memory");
}
template <int OFF> DEVI void dsr128(u32x4& d, u32 a) {
  asm volatile("ds_read_b128 %0, %1 offset:%2" : "=v"(d) : "v"(a), "n"(OFF) : "memory");
}
template <int OFF> DEVI void dsrtr(u32x2& d, u32 a) {
  asm volatile("ds_read_b64_tr_b16 %0, %1 offset:%2" : "=v"(d) : "v"(a), "n"(OFF) : "memory");
}
DEVI u32x4 cat(u32x2 a, u32x2 b) { u32x4 r = {a[0], a[1], b[0], b[1]}; return r; }
#define WAIT_V(n) asm volatile("s_waitcnt vmcnt(%0)" ::"n"(n) : "memory")
#define WAIT_L0() asm volatile("s_waitcnt lgkmcnt(0)" ::: "memory")
#define BAR() __builtin_amdgcn_s_barrier()
#define SCHED() __builtin_amdgcn_sched_barrier(0)

DEVI void ld_sc1_x4(u32x4& d, const char* base, u32 off) {
  asm volatile("global_load_dwordx4 %0, %1, %2 sc1" : "=v"(d) : "v"(off), "s"(base) : "memory");
}
DEVI void ld_x4(u32x4& d, const char* base, u32 off) {
  asm volatile("global_load_dwordx4 %0, %1, %2" : "=v"(d) : "v"(off), "s"(base) : "memory");
}
DEVI void ld_x1(u32& d, const char* base, u32 off) {
  asm volatile("global_load_dword %0, %1, %2" : "=v"(d) : "v"(off), "s"(base) : "memory");
}
DEVI void ld_u16(u32& d, const char* base, u32 off) {
  asm volatile("global_load_ushort %0, %1, %2" : "=v"(d) : "v"(off), "s"(base) : "memory");
}
DEVI u32 ld_sc1_x1_wait(const char* base, u32 off) {
  u32 d;
  asm volatile("global_load_dword %0, %1, %2 sc1\n\ts_waitcnt vmcnt(0)" : "=v"(d) : "v"(off), "s"(base) : "memory");
  return d;
}
DEVI void st_sc1_x4(char* base, u32 off, u32x4 v) {
  asm volatile("global_store_dwordx4 %0, %1, %2 sc1\n\ts_nop 1" ::"v"(off), "v"(v), "s"(base) : "memory");
}
DEVI void st_sc1_x2(char* base, u32 off, u32x2 v) {
  asm volatile("global_store_dwordx2 %0, %1, %2 sc1" ::"v"(off), "v"(v), "s"(base) : "memory");
}
DEVI void st_sc1_x1(char* base, u32 off, u32 v) {
  asm volatile("global_store_dword %0, %1, %2 sc1" ::"v"(off), "v"(v), "s"(base) : "memory");
}
constexpr int SPIN_MAX = 1 << 17;
DEVI void poll_flags(const char* base, u32 off0, int n, int l, u32 tag) {
  u32 off = off0 + (u32)(l < n ? l : 0) * 4u;
#pragma unroll 1
  for (int it = 0; it < SPIN_MAX; ++it) {
    u32 v = ld_sc1_x1_wait(base, off);
    if (__builtin_amdgcn_ballot_w64(v != tag) == 0ull) break;
    __builtin_amdgcn_s_sleep(1);
  }
  __builtin_amdgcn_fence(__ATOMIC_ACQUIRE, "wavefront");
}

#define STAMPX(i) asm volatile("s_memrealtime %0\n\ts_waitcnt lgkmcnt(0)" : "=s"(ts[i]) :: "memory")
#define STAMP(i) STAMPX(i)
#define STAMPG(i) do {} while (0)
#define STAMP_DECL u64 ts[16] = {0, 0, 0, 0, 0, 0, 0, 0, 0, 0, 0, 0, 0, 0, 0, 0}; u64 tc0, tc1; \
  asm volatile("s_memtime %0\n\ts_waitcnt lgkmcnt(0)" : "=s"(tc0) :: "memory")
#define STAMP_FLUSH do { asm volatile("s_memtime %0\n\ts_waitcnt lgkmcnt(0)" : "=s"(tc1) :: "memory"); ts[15] = tc1 - tc0; \
  if (l == 0) { _Pragma("unroll") for (int _i = 0; _i < 16; ++_i) *(u64*)(ws + WS_TIM + ((b * 8 + w) * 32 + _i) * 8) = ts[_i]; } } while (0)

DEVI void red4_max2(float& a, float& b) {
  auto s = __builtin_amdgcn_permlane32_swap(fbits(a), fbits(b), false, false);
  float t = fmaxf(bitsf(s[0]), bitsf(s[1]));
  auto u = __builtin_amdgcn_permlane16_swap(fbits(t), fbits(t), false, false);
  float m = fmaxf(bitsf(u[0]), bitsf(u[1]));
  auto z = __builtin_amdgcn_permlane32_swap(fbits(m), fbits(m), false, false);
  a = bitsf(z[0]); b = bitsf(z[1]);
}
DEVI void red4_sum2_rcp(float& a, float& b) {
  auto s = __builtin_amdgcn_permlane32_swap(fbits(a), fbits(b), false, false);
  float t = bitsf(s[0]) + bitsf(s[1]);
  auto u = __builtin_amdgcn_permlane16_swap(fbits(t), fbits(t), false, false);
  float m = __builtin_amdgcn_rcpf(bitsf(u[0]) + bitsf(u[1]));
  auto z = __builtin_amdgcn_permlane32_swap(fbits(m), fbits(m), false, false);
  a = bitsf(z[0]); b = bitsf(z[1]);
}
DEVI u32x4 dual_softmax(f32x4 p) {
  const float L2E = 1.4426950408889634f;
  float mx = fmaxf(fmaxf(p[0], p[1]), fmaxf(p[2], p[3]));
  float nm = fmaxf(fmaxf(-p[0], -p[1]), fmaxf(-p[2], -p[3]));
  red4_max2(mx, nm);
  float a = -mx * L2E, c = -nm * L2E;
  float e0 = __builtin_amdgcn_exp2f(fmaf(p[0], L2E, a)), e1 = __builtin_amdgcn_exp2f(fmaf(p[1], L2E, a));
  float e2 = __builtin_amdgcn_exp2f(fmaf(p[2], L2E, a)), e3 = __builtin_amdgcn_exp2f(fmaf(p[3], L2E, a));
  float n0 = __builtin_amdgcn_exp2f(fmaf(p[0], -L2E, c)), n1 = __builtin_amdgcn_exp2f(fmaf(p[1], -L2E, c));
  float n2 = __builtin_amdgcn_exp2f(fmaf(p[2], -L2E, c)), n3 = __builtin_amdgcn_exp2f(fmaf(p[3], -L2E, c));
  float sp = (e0 + e1) + (e2 + e3), sn = (n0 + n1) + (n2 + n3);
  red4_sum2_rcp(sp, sn);
  u32x4 r = {pk2(e0 * sp, e1 * sp), pk2(e2 * sp, e3 * sp), pk2(n0 * sn, n1 * sn), pk2(n2 * sn, n3 * sn)};
  return r;
}

template <int V> struct IC { static constexpr int value = V; };
template <int N> DEVI void wait4(u32x4& a, u32x4& b, u32x4& c, u32x4& d) {
  asm volatile("s_waitcnt vmcnt(%4)" : "+v"(a), "+v"(b), "+v"(c), "+v"(d) : "n"(N) : "memory");
}
template <int N> DEVI void wait1(u32x4& a) { asm volatile("s_waitcnt vmcnt(%1)" : "+v"(a) : "n"(N) : "memory"); }

template <int N> DEVI void waitset(u32x4& a, u32x4& b, u32x2 (&lo)[8], u32x2 (&hi)[8]);
template <> DEVI void waitset<8>(u32x4& a, u32x4& b, u32x2 (&lo)[8], u32x2 (&hi)[8]) {
  asm volatile("s_waitcnt lgkmcnt(0)"
               : "+v"(a), "+v"(b), "+v"(lo[0]), "+v"(lo[1]), "+v"(lo[2]), "+v"(lo[3]), "+v"(lo[4]), "+v"(lo[5]), "+v"(lo[6]), "+v"(lo[7]),
                 "+v"(hi[0]), "+v"(hi[1]), "+v"(hi[2]), "+v"(hi[3]), "+v"(hi[4]), "+v"(hi[5]), "+v"(hi[6]), "+v"(hi[7])
               :: "memory");
}
template <> DEVI void waitset<6>(u32x4& a, u32x4& b, u32x2 (&lo)[8], u32x2 (&hi)[8]) {
  asm volatile("s_waitcnt lgkmcnt(0)"
               : "+v"(a), "+v"(b), "+v"(lo[0]), "+v"(lo[1]), "+v"(lo[2]), "+v"(lo[3]), "+v"(lo[4]), "+v"(lo[5]),
                 "+v"(hi[0]), "+v"(hi[1]), "+v"(hi[2]), "+v"(hi[3]), "+v"(hi[4]), "+v"(hi[5])
               :: "memory");
}
DEVI void waitb(u32x2 (&lo)[4], u32x2 (&hi)[4]) {
  asm volatile("s_waitcnt lgkmcnt(0)"
               : "+v"(lo[0]), "+v"(lo[1]), "+v"(lo[2]), "+v"(lo[3]), "+v"(hi[0]), "+v"(hi[1]), "+v"(hi[2]), "+v"(hi[3]) :: "memory");
}
DEVI float wave_sum(float v) {
#define DPP_ADD(ctrl, rmask) v += bitsf((u32)__builtin_amdgcn_update_dpp(0, (int)fbits(v), ctrl, rmask, 0xf, false))
  DPP_ADD(0x111, 0xf); DPP_ADD(0x112, 0xf); DPP_ADD(0x114, 0xf); DPP_ADD(0x118, 0xf);
  DPP_ADD(0x142, 0xa); DPP_ADD(0x143, 0xc);
#undef DPP_ADD
  return bitsf((u32)__builtin_amdgcn_readlane((int)fbits(v), 63));
}

extern "C" __global__ void __launch_bounds__(512, 2)
fused_kernel(const char* __restrict__ x, const char* __restrict__ wqkv, const char* __restrict__ wphi,
             const char* __restrict__ wo, const char* __restrict__ w1, const char* __restrict__ b1,
             const char* __restrict__ w2, const char* __restrict__ b2, char* __restrict__ out, char* __restrict__ ws) {
  extern __shared__ __attribute__((aligned(16))) char smem[];
  AS3 char* L = (AS3 char*)smem;
  asm volatile("" ::"s"(x), "s"(wqkv), "s"(wphi), "s"(wo), "s"(w1), "s"(b1), "s"(w2), "s"(b2), "s"(out), "s"(ws));
  const int b = blockIdx.x;
  const int l = threadIdx.x & 63;
  const int w = __builtin_amdgcn_readfirstlane(threadIdx.x >> 6);
  const int xcd = b & 7, bi = b >> 3;
  const int p = (xcd >> 2) * 8 + (bi >> 2), c = (xcd & 3) * 4 + (bi & 3), g = c >> 1;
  const bool typeA = (c & 1) == 0;
  const int rg = w & 3, cg = w >> 2;
  const int gq = l >> 4, r16 = l & 15, q4 = (l >> 2) & 3, pp = l & 3;
  const int rot = (p + c) & 7;
  const int rb = 4 * xcd + (bi >> 3), cb = bi & 7;
  STAMP_DECL;
  STAMP(0);

  const u32 lb = (u32)(size_t)L;
  const u32 xrow_off = (u32)(128 * p + 8 * c + w) * 1024u + (u32)l * 16u;
  u32 gb1[2];
  const u32 gb1_off = (u32)(224 * g + (w < 6 ? 32 * w + ((g & 1) ? 32 : 0) : (w == 6 && !(g & 1) ? 192 : 0)) + (l >> 2)) * 2u;
  u32 tag;

  const char* xsrc = x + (size_t)p * 131072u;
  const u32 wstride = typeA ? 3072u : 3584u;
  const char* wsrc = (typeA ? (wqkv + g * 128) : (w1 + 448 * g)) - 4096;
  const u32 vx0 = (u32)(16 * w + (l >> 3)) * 1024u + (u32)(((l & 7) ^ (l >> 4)) * 16);
  const u32 vx1 = (u32)(16 * w + 7 + (l >> 3)) * 1024u + (u32)(((l & 7) ^ (4 + (l >> 4))) * 16);
  const u32 wsp = (u32)(((l & 7) >> 1) ^ (((l >> 4) & 1) | ((w & 1) << 1)));
  const u32 vw = (u32)(8 * w + (l >> 3)) * wstride + (wsp * 2u + (u32)(l & 1)) * 16u + 4096u;
  u32 vws[4];
  {
    const u32 odd = (u32)(g & 1) * 64u;
    vws[0] = typeA ? vw : vw + odd;
    vws[1] = typeA ? vw : vw + odd + 128u - 1024u;
    vws[2] = typeA ? vw : vw + odd + 256u - 2048u;
    vws[3] = vw + (odd ? 0u : 384u) - 3072u;
  }
  const u64 hmask = __builtin_amdgcn_ballot_w64(wsp < 2u);
  const u32 m0x = lb + (u32)w * 2048u, m0w = lb + 16384u + (u32)w * 4096u;

  const char* dsrc2 = w2 + (size_t)(7 * w) * 32768u + cb * 128;
  const char* dsrco = wo + (size_t)(2 * w) * 32768u + cb * 128;
  const u32 vdb = (u32)(l >> 3) * 1024u + (u32)(((((l & 7) >> 1) ^ ((l >> 4) & 3)) * 2 + (l & 1)) * 16);
  auto dslot = [&](int j) -> u32 { return (u32)((j + 3) & 3) * 32768u + (u32)w * 4096u; };
  auto issueB = [&](int j) {
    const char* s = (j < 7) ? (dsrc2 + j * 32768) : (dsrco + (j - 7) * 32768);
    glds_m0<0>(lb + dslot(j), s, vdb);
    glds<1024>(s, vdb + 7168u);
    glds<2048>(s, vdb + 2u * 7168u);
    glds<3072>(s, vdb + 3u * 7168u);
  };

  u32 xa0 = lb + (u32)(32 * rg + r16) * 128u + (u32)((gq ^ ((r16 >> 1) & 7)) * 16);
  u32 xa1 = xa0 ^ 64u;
  const u32 vbase = lb + 16384u + (u32)gq * 4096u + (u32)q4 * 128u + (u32)pp * 8u + (u32)(((q4 >> 1) & 1) | ((gq & 1) << 1)) * 32u;
  u32 R[8];
#pragma unroll
  for (int t = 0; t < 8; ++t) {
    const u32 ra = (vbase ^ (u32)((2 * cg + (t & 1)) * 32)) + (u32)(t >> 1) * 1024u;
    const u32 rbb = (vbase ^ (u32)((t & 3) * 32)) + (u32)(t >> 2) * 1024u + (u32)cg * 2048u;
    R[t] = typeA ? ra : rbb;
  }

  f32x4 acc[8][2];
#pragma unroll
  for (int t = 0; t < 8; ++t) { acc[t][0] = f32x4{0, 0, 0, 0}; acc[t][1] = f32x4{0, 0, 0, 0}; }
  u32x4 gat[8];
  const u32x4 ones = {0x3F803F80u, 0x3F803F80u, 0x3F803F80u, 0x3F803F80u};
  const u32 gcs = WS_CS + (u32)(c * 256 + 32 * w + (l >> 2)) * 64u + (u32)(l & 3) * 16u;
  const u32 gst = WS_STAT + (u32)(128 * p + 16 * w + r16) * 16u;

  auto g1 = [&](auto TAc, auto N8c) {
    constexpr bool TA = decltype(TAc)::value != 0, N8 = decltype(N8c)::value != 0;
    constexpr int NG = TA ? 5 : 6;
    u32x4 fx[2][2];
    u32x2 flo[2][8], fhi[2][8];
    u32x4 xrow;
    u32 epoch;
    asm volatile("s_load_dword %0, %1, 0x0" : "=s"(epoch) : "s"(ws) : "memory");
    ld_x4(xrow, x, xrow_off);
    if (TA) { gb1[0] = 0; gb1[1] = 0; }
    else { ld_u16(gb1[0], b1, gb1_off); ld_u16(gb1[1], b1, gb1_off + 32u); }
    auto issue = [&](int s, u32 sb) {
      const int ks = (s + rot) & 7;
      const char* xs = xsrc + ks * 128;
      const char* wsb = wsrc + (size_t)((u32)ks * 64u * wstride);
      glds_m0<0>(m0x + sb, xs, vx0);
      glds<1024>(xs, vx1);
      glds_m0<0>(m0w + sb, wsb, vws[0]);
      glds<1024>(wsb, vws[1]);
      glds<2048>(wsb, vws[2]);
      if (!TA) glds_masked<3072>(wsb, vws[3], hmask);
    };
    issue(0, 0); issue(1, SLOT); issue(2, 2 * SLOT);
    if (TA && w < 2) glds_m0<0>(lb + L_WPHI + (u32)w * 1024u, wphi + g * 2048 + w * 1024, (u32)l * 16u);

#define MF(S, t, rt) acc[t][rt] = mfma32(cat(flo[S][t], fhi[S][t]), fx[S][rt], acc[t][rt])
#define MFS(S, t, rt) acc[t][rt] = mfma32(fx[S][rt], cat(flo[S][t], fhi[S][t]), acc[t][rt])
#define RW(N, t) do { if (RD) { dsrtr<(N) * 16384>(flo[N][t], R[t]); dsrtr<(N) * 16384 + 512>(fhi[N][t], R[t]); } } while (0)
#define RX(N) do { if (RD) { dsr128<0>(fx[N][0], (N) ? xa1 : xa0); dsr128<2048>(fx[N][1], (N) ? xa1 : xa0); } } while (0)
    auto waitS = [&](auto Sc) {
      constexpr int S = decltype(Sc)::value;
      if constexpr (N8) waitset<8>(fx[S][0], fx[S][1], flo[S], fhi[S]);
      else waitset<6>(fx[S][0], fx[S][1], flo[S], fhi[S]);
    };
    auto phase = [&](auto Sc, auto RDc, auto GLc, const char* xs, const char* wsb, u32 sb) {
      constexpr int S = decltype(Sc)::value, N = 1 - S;
      constexpr bool RD = decltype(RDc)::value != 0, GL = decltype(GLc)::value != 0;
      waitS(Sc);
      if (S == 1 && RD) BAR();
      SCHED();
      if constexpr (N8) {
        MF(S, 6, 0); RW(N, 6); SCHED();
        MF(S, 6, 1); RW(N, 7); SCHED();
        MF(S, 7, 0); SCHED();
        MF(S, 7, 1); SCHED();
      }
      if constexpr (TA) {
        MFS(S, 4, 0); RX(N); SCHED();
        MFS(S, 4, 1); RW(N, 4); SCHED();
        MFS(S, 5, 0); RW(N, 5); SCHED();
        MFS(S, 5, 1); RW(N, 0); SCHED();
      } else {
        MF(S, 4, 0); RX(N); SCHED();
        MF(S, 4, 1); RW(N, 4); SCHED();
        MF(S, 5, 0); RW(N, 5); SCHED();
        MF(S, 5, 1); RW(N, 0); SCHED();
      }
      MF(S, 0, 0); RW(N, 1); SCHED();
      MF(S, 0, 1); RW(N, 2); SCHED();
      MF(S, 1, 0); RW(N, 3); SCHED();
      MF(S, 1, 1); if (GL) { glds_m0<0>(m0x + sb, xs, vx0); glds<1024>(xs, vx1); } SCHED();
      MF(S, 2, 0); if (GL) glds_m0<0>(m0w + sb, wsb, vws[0]); SCHED();
      MF(S, 2, 1); if (GL) glds<1024>(wsb, vws[1]); SCHED();
      MF(S, 3, 0); if (GL) glds<2048>(wsb, vws[2]); SCHED();
      MF(S, 3, 1); if (GL && !TA) glds_masked<3072>(wsb, vws[3], hmask); SCHED();
    };
    auto advance = [&](u32 d) {
#pragma unroll
      for (int t = 0; t < (N8 ? 8 : 6); ++t) R[t] += d;
      xa0 += d; xa1 += d;
    };

    STAMP(1);
    if (TA) asm volatile("s_waitcnt vmcnt(%1)" : "+v"(xrow) : "n"(2 * NG) : "memory");
    else asm volatile("s_waitcnt vmcnt(%3)" : "+v"(xrow), "+v"(gb1[0]), "+v"(gb1[1]) : "n"(2 * NG) : "memory");
    STAMPG(1);
    BAR();
    STAMPG(2);
    {
      constexpr bool RD = true;
      RX(0); RW(0, 4); RW(0, 5); RW(0, 0); RW(0, 1); RW(0, 2); RW(0, 3);
      if constexpr (N8) { RW(0, 6); RW(0, 7); }
    }
    waitS(IC<0>{});
    asm volatile("" : "+s"(epoch) :: "memory");
    tag = epoch + 1u;
    phase(IC<0>{}, IC<1>{}, IC<0>{}, nullptr, nullptr, 0u);
    {
      float v[8];
#pragma unroll
      for (int i = 0; i < 4; ++i) { v[2 * i] = bflo(xrow[i]); v[2 * i + 1] = bfhi(xrow[i]); }
      const float mean = wave_sum(((v[0] + v[1]) + (v[2] + v[3])) + ((v[4] + v[5]) + (v[6] + v[7]))) * (1.0f / 512.0f);
      float qq = 0.f;
#pragma unroll
      for (int i = 0; i < 8; ++i) { float d = v[i] - mean; qq = fmaf(d, d, qq); }
      const float rstd = 1.0f / sqrtf(wave_sum(qq) * (1.0f / 512.0f) + 1e-5f);
      if (l == 0) {
        u32x4 gr = {fbits(mean), fbits(rstd), tag, tag};
        st_sc1_x4(ws, WS_STAT + (u32)(128 * p + 8 * c + w) * 16u, gr);
        if (w == 7) st_sc1_x1(ws, WS_AFLAG + (u32)b * 4u, tag);
      }
      if (p < 8 && w < (TA ? 6 : 7)) {
        u32x2 c0l[2], c0h[2], c1l[2], c1h[2];
        const u32 a0 = (vbase ^ (u32)((2 * (w & 1)) * 32)) + (u32)(w >> 1) * 1024u;
        const u32 a1 = a0 ^ 32u;
        dsrtr<0>(c0l[0], a0); dsrtr<512>(c0h[0], a0); dsrtr<16384>(c0l[1], a0); dsrtr<16384 + 512>(c0h[1], a0);
        dsrtr<0>(c1l[0], a1); dsrtr<512>(c1h[0], a1); dsrtr<16384>(c1l[1], a1); dsrtr<16384 + 512>(c1h[1], a1);
        asm volatile("s_waitcnt lgkmcnt(0)" : "+v"(c0l[0]), "+v"(c0h[0]), "+v"(c0l[1]), "+v"(c0h[1]),
                     "+v"(c1l[0]), "+v"(c1h[0]), "+v"(c1l[1]), "+v"(c1h[1]) :: "memory");
        f32x4 c0 = {0, 0, 0, 0}, c1 = {0, 0, 0, 0};
#pragma unroll
        for (int ks = 0; ks < 2; ++ks) {
          c0 = mfma32(ones, cat(c0l[ks], c0h[ks]), c0);
          c1 = mfma32(ones, cat(c1l[ks], c1h[ks]), c1);
        }
        if (l < 32) {
          u32x2 gr = {fbits(l < 16 ? c0[0] : c1[0]), tag};
          st_sc1_x2(ws, WS_CS + (u32)((c * 256 + 32 * w + l) * 8 + rot) * 8u, gr);
        }
      }
    }
    u32 sb = 0;
#pragma unroll 1
    for (int s = 0; s < 5; ++s) {
      const u32 nsb = sb == 2 * SLOT ? 0u : sb + SLOT;
      advance(nsb - sb);
      const int ks = (s + 3 + rot) & 7;
      WAIT_V(NG);
      STAMPG(3 + s);
      phase(IC<1>{}, IC<1>{}, IC<EXP_GL>{}, xsrc + ks * 128, wsrc + (size_t)((u32)ks * 64u * wstride), sb);
      phase(IC<0>{}, IC<1>{}, IC<0>{}, nullptr, nullptr, 0u);
      sb = nsb;
    }
    advance(0u - 2u * SLOT);
    WAIT_V(NG);
    STAMPG(8);
    phase(IC<1>{}, IC<1>{}, IC<0>{}, nullptr, nullptr, 0u);
    ld_sc1_x4(gat[0], ws, gcs);
    ld_sc1_x4(gat[1], ws, gcs + 1024u);
    ld_sc1_x4(gat[2], ws, gst);
    issueB(0);
    phase(IC<0>{}, IC<1>{}, IC<0>{}, nullptr, nullptr, 0u);
    advance(SLOT);
    WAIT_V(7);
    STAMPG(9);
    phase(IC<1>{}, IC<1>{}, IC<0>{}, nullptr, nullptr, 0u);
    issueB(1);
    phase(IC<0>{}, IC<1>{}, IC<0>{}, nullptr, nullptr, 0u);
    phase(IC<1>{}, IC<0>{}, IC<0>{}, nullptr, nullptr, 0u);
#undef MF
#undef MFS
#undef RW
#undef RX
    STAMP(2);
    STAMPG(10);
    asm volatile("s_waitcnt vmcnt(8)" : "+v"(gat[0]), "+v"(gat[1]), "+v"(gat[2]) :: "memory");
    STAMPG(11);
  };
  if (typeA) g1(IC<1>{}, IC<0>{});
  else if (cg == 0) g1(IC<0>{}, IC<1>{});
  else g1(IC<0>{}, IC<0>{});
  {
    const bool used = w < (typeA ? 6 : 7);
  #pragma unroll 1
  for (int it = 0; it < SPIN_MAX; ++it) {
      const bool bad = (used && ((gat[0][1] != tag) | (gat[0][3] != tag) | (gat[1][1] != tag) | (gat[1][3] != tag))) | (gat[2][2] != tag);
      if (__builtin_amdgcn_ballot_w64(bad) == 0ull) break;
      ld_sc1_x4(gat[0], ws, gcs);
      ld_sc1_x4(gat[1], ws, gcs + 1024u);
      ld_sc1_x4(gat[2], ws, gst);
      asm volatile("s_waitcnt vmcnt(0)" : "+v"(gat[0]), "+v"(gat[1]), "+v"(gat[2]) :: "memory");
    }
#pragma unroll
    for (int i = 0; i < 2; ++i) {
      float sm = bitsf(gat[i][0]) + bitsf(gat[i][2]);
      sm += bitsf((u32)__builtin_amdgcn_update_dpp(0, (int)fbits(sm), 0xB1, 0xf, 0xf, false));
      sm += bitsf((u32)__builtin_amdgcn_update_dpp(0, (int)fbits(sm), 0x4E, 0xf, 0xf, false));
      if ((l & 3) == 0) {
        f32x2 o = {sm, bflo(gb1[i])};
        *(AS3 f32x2*)(L + L_CSB + (u32)(32 * w + 16 * i + (l >> 2)) * 8u) = o;
      }
    }
    if (l < 16) {
      *(AS3 float*)(L + L_MEAN + (u32)(16 * w + l) * 4u) = bitsf(gat[2][0]);
      *(AS3 float*)(L + L_RSTD + (u32)(16 * w + l) * 4u) = bitsf(gat[2][1]);
    }
  }
  WAIT_L0();
  BAR();
  issueB(2); issueB(3);
  STAMP(3);

  const float mean0 = *(AS3 float*)(L + L_MEAN + (u32)(32 * rg + r16) * 4u);
  const float mean1 = *(AS3 float*)(L + L_MEAN + (u32)(32 * rg + 16 + r16) * 4u);
  const float rstd0 = *(AS3 float*)(L + L_RSTD + (u32)(32 * rg + r16) * 4u);
  const float rstd1 = *(AS3 float*)(L + L_RSTD + (u32)(32 * rg + 16 + r16) * 4u);

  const u32 vb2 = (u32)(4 * gq + q4) * 128u + (u32)pp * 8u + (u32)((2 * gq + (q4 >> 1)) & 3) * 32u;
  u32x2 blo[4][4], bhi[4][4];
  u32 VB[4];
#pragma unroll
  for (int ct = 0; ct < 4; ++ct) VB[ct] = lb + (vb2 ^ (u32)(ct * 32)) + (u32)w * 4096u;
  auto rdB = [&](auto jc) {
    constexpr int j = decltype(jc)::value, blk = (j + 3) & 3;
#pragma unroll
    for (int ct = 0; ct < 4; ++ct) {
      const u32 a = VB[ct] + (blk >= 2 ? 65536u : 0u);
      dsrtr<(blk & 1) * 32768>(blo[j & 3][ct], a);
      dsrtr<(blk & 1) * 32768 + 2048>(bhi[j & 3][ct], a);
    }
  };
  auto loadBreg = [&]() {
    rdB(IC<0>{}); rdB(IC<1>{}); waitb(blo[0], bhi[0]); waitb(blo[1], bhi[1]);
    rdB(IC<2>{}); rdB(IC<3>{}); waitb(blo[2], bhi[2]); waitb(blo[3], bhi[3]);
    issueB(4); issueB(5); issueB(6); issueB(7);
  };

  if (!typeA) {
    auto hpair = [&](auto Pc) {
      constexpr int P = decltype(Pc)::value;
      const int hl = cg * 4 + P;
      u32x4 o0, o1;
#pragma unroll
      for (int e = 0; e < 2; ++e) {
        const int t = 2 * P + e;
        const u32 ca = L_CSB + (u32)(hl * 32 + e * 16 + 4 * gq) * 8u;
        const f32x4 cb0 = lds128f(L, ca), cb1 = lds128f(L, ca + 16u);
        const float cs[4] = {cb0[0], cb0[2], cb1[0], cb1[2]}, bb[4] = {cb0[1], cb0[3], cb1[1], cb1[3]};
        float h0[4], h1[4];
#pragma unroll
        for (int r = 0; r < 4; ++r) {
          float y0 = fmaf(rstd0, fmaf(-mean0, cs[r], acc[t][0][r]), bb[r]);
          float y1 = fmaf(rstd1, fmaf(-mean1, cs[r], acc[t][1][r]), bb[r]);
          y0 = fmaxf(y0, 0.f); y1 = fmaxf(y1, 0.f);
          h0[r] = y0 * y0; h1[r] = y1 * y1;
        }
        o0[2 * e] = pk2(h0[0], h0[1]); o0[2 * e + 1] = pk2(h0[2], h0[3]);
        o1[2 * e] = pk2(h1[0], h1[1]); o1[2 * e + 1] = pk2(h1[2], h1[3]);
      }
      const u32 step = (u32)(7 * g + ((g & 1) ? (hl < 6 ? hl + 1 : 0) : hl));
      const u32 off = WS_AF + ((u32)(8 * p + 2 * rg) * 72u + step) * 1024u + (u32)l * 16u;
      st_sc1_x4(ws, off, o0);
      st_sc1_x4(ws, off + 72u * 1024u, o1);
    };
    hpair(IC<0>{}); hpair(IC<1>{}); hpair(IC<2>{});
    if (cg == 0) hpair(IC<3>{});
    WAIT_V(0);
    BAR();
    if (threadIdx.x == 0) st_sc1_x1(ws, WS_HFLAG + (u32)p * 128u + (u32)g * 4u, tag);
    STAMP(4);
    loadBreg();
  } else {
    const u32x2 ident = {(4 * gq + 0 == r16 ? 0x3F80u : 0u) | (4 * gq + 1 == r16 ? 0x3F800000u : 0u),
                         (4 * gq + 2 == r16 ? 0x3F80u : 0u) | (4 * gq + 3 == r16 ? 0x3F800000u : 0u)};
    const f32x4 zero4 = {0, 0, 0, 0};
    const f32x4 vm0 = lds128f(L, L_MEAN + (u32)(32 * rg + 4 * gq) * 4u), vm1 = lds128f(L, L_MEAN + (u32)(32 * rg + 16 + 4 * gq) * 4u);
    const f32x4 vr0 = lds128f(L, L_RSTD + (u32)(32 * rg + 4 * gq) * 4u), vr1 = lds128f(L, L_RSTD + (u32)(32 * rg + 16 + 4 * gq) * 4u);
    u32x2 wp[2];
    auto logits = [&](int line, int pi, int rt) -> f32x4 {
      const int t = line * 2 + pi;
      const u32 ca = L_CSB + (u32)(line * 64 + (2 * cg + pi) * 16 + 4 * gq) * 8u;
      const f32x4 cb0 = lds128f(L, ca), cb1 = lds128f(L, ca + 16u);
      const float mn = rt ? mean1 : mean0, rs = rt ? rstd1 : rstd0;
      const float y0 = rs * fmaf(-mn, cb0[0], acc[t][rt][0]), y1 = rs * fmaf(-mn, cb0[2], acc[t][rt][1]);
      const float y2 = rs * fmaf(-mn, cb1[0], acc[t][rt][2]), y3 = rs * fmaf(-mn, cb1[2], acc[t][rt][3]);
      const u32x2 kb = {pk2(y0, y1), pk2(y2, y3)};
      return mfma16(wp[pi], kb, zero4);
    };
#pragma unroll
    for (int pi = 0; pi < 2; ++pi)
      wp[pi] = lds_tr(L, L_WPHI + (u32)((2 * cg + pi) * 512 + (4 * gq + q4) * 32 + pp * 8));
    f32x4 kvt[4], zt[4];
#pragma unroll
    for (int pi = 0; pi < 2; ++pi) {
      u32x4 tp, tn, vv;
#pragma unroll
      for (int rt = 0; rt < 2; ++rt) {
        const u32x4 ph = dual_softmax(logits(1, pi, rt));
        const f32x4 a = mfma16(u32x2{ph[0], ph[1]}, ident, zero4);
        const f32x4 n = mfma16(u32x2{ph[2], ph[3]}, ident, zero4);
        tp[2 * rt] = pk2(a[0], a[1]); tp[2 * rt + 1] = pk2(a[2], a[3]);
        tn[2 * rt] = pk2(n[0], n[1]); tn[2 * rt + 1] = pk2(n[2], n[3]);
        const float cs = *(AS3 float*)(L + L_CSB + (u32)(128 + (2 * cg + pi) * 16 + r16) * 8u);
        const f32x4 vm = rt ? vm1 : vm0, vr = rt ? vr1 : vr0;
        const f32x4 av = acc[4 + pi][rt];
        vv[2 * rt] = pk2(vr[0] * fmaf(-vm[0], cs, av[0]), vr[1] * fmaf(-vm[1], cs, av[1]));
        vv[2 * rt + 1] = pk2(vr[2] * fmaf(-vm[2], cs, av[2]), vr[3] * fmaf(-vm[3], cs, av[3]));
      }
      kvt[2 * pi] = mfma32(tp, vv, zero4); kvt[2 * pi + 1] = mfma32(tn, vv, zero4);
      zt[2 * pi] = mfma32(tp, ones, zero4); zt[2 * pi + 1] = mfma32(tn, ones, zero4);
    }
    {
      const int sl = rg > 0 ? rg - 1 : 0;
#pragma unroll
      for (int pi = 0; pi < 2; ++pi) {
        if (rg != pi) {
#pragma unroll
          for (int sg = 0; sg < 2; ++sg)
            *(AS3 f32x4*)(L + L_SCR + (u32)((((cg * 2 + pi) * 2 + sg) * 3 + sl) * 1024 + l * 16)) = kvt[2 * pi + sg];
        }
        if (r16 == 0) {
#pragma unroll
          for (int sg = 0; sg < 2; ++sg)
            *(AS3 f32x4*)(L + L_ZR + (u32)(((((cg * 2 + pi) * 2 + sg) * 4 + rg) * 16 + 4 * gq) * 4)) = zt[2 * pi + sg];
        }
      }
    }
    WAIT_L0();
    BAR();
    if (rg < 2) {
      f32x4 sp = rg == 0 ? kvt[0] : kvt[2], sn = rg == 0 ? kvt[1] : kvt[3];
#pragma unroll
      for (int sl = 0; sl < 3; ++sl) {
        sp += lds128f(L, L_SCR + (u32)((((cg * 2 + rg) * 2 + 0) * 3 + sl) * 1024 + l * 16));
        sn += lds128f(L, L_SCR + (u32)((((cg * 2 + rg) * 2 + 1) * 3 + sl) * 1024 + l * 16));
      }
      u32x4 o = {pk2(sp[0], sp[1]), pk2(sp[2], sp[3]), pk2(sn[0], sn[1]), pk2(sn[2], sn[3])};
      st_sc1_x4(ws, WS_KVP + (u32)(((g * 16 + p) * 4 + 2 * cg + rg) * 1024 + l * 16), o);
    } else if (l < 8) {
      const int pi = rg - 2, sg = l >> 2, qq = l & 3;
      f32x4 s = {0, 0, 0, 0};
#pragma unroll
      for (int r = 0; r < 4; ++r) s += lds128f(L, L_ZR + (u32)(((((cg * 2 + pi) * 2 + sg) * 4 + r) * 16 + 4 * qq) * 4));
      st_sc1_x4(ws, WS_ZP + (u32)((((g * 4 + 2 * cg + pi) * 16 + p) * 32 + l * 4) * 4), __builtin_bit_cast(u32x4, s));
    }
    WAIT_V(0);
    BAR();
    if (threadIdx.x == 0) st_sc1_x1(ws, WS_KVFLAG + (u32)g * 128u + (u32)p * 4u, tag);
    STAMP(4);
    loadBreg();
    u32x4 phq[2][2];
#pragma unroll
    for (int pi = 0; pi < 2; ++pi)
#pragma unroll
      for (int rt = 0; rt < 2; ++rt) phq[pi][rt] = dual_softmax(logits(0, pi, rt));
    STAMP(5);
    if (w == 0) poll_flags(ws, WS_KVFLAG + (u32)g * 128u, 16, l, tag);
    BAR();
    STAMP(6);
    {
      const int hd = w & 3, hf = w >> 2;
#pragma unroll
      for (int i = 0; i < 8; ++i) ld_sc1_x4(gat[i], ws, WS_KVP + (u32)(((g * 16 + 8 * hf + i) * 4 + hd) * 1024 + l * 16));
      u32x4 zg;
      ld_sc1_x4(zg, ws, WS_ZP + (u32)(((g * 4 + hd) * 16 + 8 * hf) * 128 + l * 16));
      asm volatile("s_waitcnt vmcnt(0)" : "+v"(gat[0]), "+v"(gat[1]), "+v"(gat[2]), "+v"(gat[3]), "+v"(gat[4]), "+v"(gat[5]),
                   "+v"(gat[6]), "+v"(gat[7]), "+v"(zg) :: "memory");
      f32x4 s0 = {0, 0, 0, 0}, s1 = {0, 0, 0, 0};
#pragma unroll
      for (int i = 0; i < 8; ++i) {
        s0[0] += bflo(gat[i][0]); s0[1] += bfhi(gat[i][0]); s0[2] += bflo(gat[i][1]); s0[3] += bfhi(gat[i][1]);
        s1[0] += bflo(gat[i][2]); s1[1] += bfhi(gat[i][2]); s1[2] += bflo(gat[i][3]); s1[3] += bfhi(gat[i][3]);
      }
      *(AS3 f32x4*)(L + L_SCR + (u32)((hd * 2 + hf) * 2048 + l * 32)) = s0;
      *(AS3 f32x4*)(L + L_SCR + (u32)((hd * 2 + hf) * 2048 + l * 32 + 16)) = s1;
      f32x4 zs = __builtin_bit_cast(f32x4, zg);
#pragma unroll
      for (int r = 0; r < 4; ++r) {
        float v = zs[r];
        v += __shfl_xor(v, 8); v += __shfl_xor(v, 16); v += __shfl_xor(v, 32);
        zs[r] = v;
      }
      if (l < 8) *(AS3 f32x4*)(L + L_ZR + (u32)(((hd * 2 + hf) * 32 + l * 4) * 4)) = zs;
    }
    WAIT_L0();
    BAR();
    {
      u32x4 o0, o1;
#pragma unroll
      for (int pi = 0; pi < 2; ++pi) {
        const int hd = 2 * cg + pi;
        const u32 ga = L_SCR + (u32)(hd * 4096 + l * 32);
        const f32x4 kp = lds128f(L, ga) + lds128f(L, ga + 2048u), kn = lds128f(L, ga + 16u) + lds128f(L, ga + 2064u);
        const u32 za = L_ZR + (u32)((hd * 64 + 4 * gq) * 4);
        const f32x4 zp = lds128f(L, za) + lds128f(L, za + 128u), zn = lds128f(L, za + 64u) + lds128f(L, za + 192u);
        const u32x4 kf = {pk2(kp[0], kp[1]), pk2(kp[2], kp[3]), pk2(kn[0], kn[1]), pk2(kn[2], kn[3])};
        const u32x4 zf = {pk2(zp[0], zp[1]), pk2(zp[2], zp[3]), pk2(zn[0], zn[1]), pk2(zn[2], zn[3])};
#pragma unroll
        for (int rt = 0; rt < 2; ++rt) {
          const f32x4 num = mfma32(kf, phq[pi][rt], zero4), den = mfma32(zf, phq[pi][rt], zero4);
          const float rd = __builtin_amdgcn_rcpf(den[0] + 1e-6f);
          const u32 a = pk2(num[0] * rd, num[1] * rd), bq = pk2(num[2] * rd, num[3] * rd);
          if (rt == 0) { o0[2 * pi] = a; o0[2 * pi + 1] = bq; } else { o1[2 * pi] = a; o1[2 * pi + 1] = bq; }
        }
      }
      const u32 off = WS_AF + ((u32)(8 * p + 2 * rg) * 72u + (u32)(56 + 2 * g + cg)) * 1024u + (u32)l * 16u;
      st_sc1_x4(ws, off, o0);
      st_sc1_x4(ws, off + 72u * 1024u, o1);
    }
    WAIT_V(0);
    BAR();
    if (threadIdx.x == 0) st_sc1_x1(ws, WS_OFLAG + (u32)p * 128u + (u32)g * 4u, tag);
    STAMP(7);
  }

  {
    const int dp = rb >> 1;
    u32x4 xres, b2v;
    const u32 fo = (u32)(64 * rb + (threadIdx.x >> 3)) * 1024u + (u32)(cb * 128 + (threadIdx.x & 7) * 16);
    if (w == 0) poll_flags(ws, WS_HFLAG + (u32)dp * 128u, 8, l, tag);
    BAR();
    STAMP(8);
    const char* af = ws + WS_AF + (size_t)(4 * rb) * 73728u;
    const char* afh = af + (size_t)(7 * w) * 1024u;
    const char* afo = af + (size_t)(56 + 2 * w) * 1024u;
    const u32 vo = (u32)l * 16u;
    f32x4 dacc[4][4];
#pragma unroll
    for (int i = 0; i < 4; ++i)
#pragma unroll
      for (int j = 0; j < 4; ++j) dacc[i][j] = f32x4{0, 0, 0, 0};
    u32x4 a[9][4];
    auto loadA = [&](int j) {
      const char* s = j < 7 ? afh + j * 1024 : afo + (j - 7) * 1024;
#pragma unroll
      for (int rt = 0; rt < 4; ++rt) ld_sc1_x4(a[j][rt], s + rt * 73728, vo);
    };
    auto waitA = [&](int j, auto n) { wait4<decltype(n)::value>(a[j][0], a[j][1], a[j][2], a[j][3]); };
    auto step = [&](int j) {
#pragma unroll
      for (int ct = 0; ct < 4; ++ct) {
        const u32x4 bf = cat(blo[j & 3][ct], bhi[j & 3][ct]);
#pragma unroll
        for (int rt = 0; rt < 4; ++rt) dacc[rt][ct] = mfma32(bf, a[j][rt], dacc[rt][ct]);
      }
      SCHED();
    };
    loadA(0); loadA(1); loadA(2); loadA(3);
    ld_x4(xres, x, fo);
    waitA(0, IC<13>{}); step(0); rdB(IC<4>{}); loadA(4);
    waitA(1, IC<13>{}); waitb(blo[0], bhi[0]); issueB(8); step(1); rdB(IC<5>{}); loadA(5);
    waitA(2, IC<17>{}); step(2); rdB(IC<6>{}); loadA(6);
    waitA(3, IC<17>{}); step(3); rdB(IC<7>{});
    waitA(4, IC<12>{}); step(4);
    waitA(5, IC<4>{}); waitb(blo[1], bhi[1]); step(5); rdB(IC<8>{});
    waitA(6, IC<0>{}); waitb(blo[2], bhi[2]); step(6);
    STAMP(9);
    poll_flags(ws, WS_OFLAG + (u32)dp * 128u + (u32)w * 4u, 1, l, tag);
    STAMP(10);
    loadA(7); loadA(8);
    ld_x4(b2v, b2, (u32)(cb * 128 + (threadIdx.x & 7) * 16));
    waitA(7, IC<5>{}); waitb(blo[3], bhi[3]); step(7);
    waitA(8, IC<1>{}); waitb(blo[0], bhi[0]); step(8);
    WAIT_L0();
#pragma unroll
    for (int rt = 0; rt < 4; ++rt)
#pragma unroll
      for (int ct = 0; ct < 4; ++ct)
        *(AS3 f32x4*)(L + dslot(rt) + (u32)(r16 * 256 + (((ct * 4 + gq) ^ r16) * 16))) = dacc[rt][ct];
    WAIT_L0();
    BAR();
    STAMP(11);
    {
      const int row = threadIdx.x >> 3, cq = threadIdx.x & 7, rt = row >> 4, tk = row & 15;
      const u32 pb = (u32)((rt + 3) & 3) * 32768u + (u32)(tk * 256);
      f32x4 s0 = {0, 0, 0, 0}, s1 = {0, 0, 0, 0};
#pragma unroll
      for (int ww = 0; ww < 8; ++ww) {
        s0 += lds128f(L, pb + (u32)(ww * 4096) + (u32)(((2 * cq) ^ tk) * 16));
        s1 += lds128f(L, pb + (u32)(ww * 4096) + (u32)(((2 * cq + 1) ^ tk) * 16));
      }
      asm volatile("s_waitcnt vmcnt(0)" : "+v"(xres), "+v"(b2v) :: "memory");
      u32x4 o;
      o[0] = pk2(s0[0] + bflo(xres[0]) + bflo(b2v[0]), s0[1] + bfhi(xres[0]) + bfhi(b2v[0]));
      o[1] = pk2(s0[2] + bflo(xres[1]) + bflo(b2v[1]), s0[3] + bfhi(xres[1]) + bfhi(b2v[1]));
      o[2] = pk2(s1[0] + bflo(xres[2]) + bflo(b2v[2]), s1[1] + bfhi(xres[2]) + bfhi(b2v[2]));
      o[3] = pk2(s1[2] + bflo(xres[3]) + bflo(b2v[3]), s1[3] + bfhi(xres[3]) + bfhi(b2v[3]));
      *(u32x4*)(out + fo) = o;
    }
    if (b == 255 && w == 7) {
#pragma unroll 1
      for (int i = 0; i < 4; ++i) poll_flags(ws, WS_AFLAG + (u32)i * 256u, 64, l, tag);
      if (l == 0) st_sc1_x1(ws, WS_EPOCH, tag);
    }
    STAMP(12);
    STAMP_FLUSH;
  }
}

__global__ void stampA(char* ws) {
  u64 now; asm volatile("s_memrealtime %0\n\ts_waitcnt lgkmcnt(0)" : "=s"(now) :: "memory");
  if (threadIdx.x == 0) *(u64*)(ws + WS_TIM - 64) = now;
}
__global__ void report(const char* ws) {
  u64 start; asm volatile("s_memrealtime %0\n\ts_waitcnt lgkmcnt(0)" : "=s"(start) :: "memory");
  const u64* tm = (const u64*)(ws + WS_TIM);
  const int l = threadIdx.x;
  u64 t0 = ~0ull;
  for (int i = l; i < 2048; i += 64) { u64 t = tm[i * 32]; t0 = t < t0 ? t : t0; }
  for (int o = 32; o; o >>= 1) { u64 v = __shfl_xor(t0, o); t0 = v < t0 ? v : t0; }
  u64 ta = *(const u64*)(ws + WS_TIM - 64);
  u64 now; asm volatile("s_memrealtime %0\n\ts_waitcnt lgkmcnt(0)" : "=s"(now) : "v"(t0), "v"(ta) : "memory");
  float v = MODE == 1 ? (float)(t0 - ta) : MODE == 2 ? (float)(start - t0) : MODE == 3 ? (float)(now - start) : 0.f;
  if (v > 100000.f || v < 0.f) v = 0.f;
  const u64 end = now + (u64)(v * 20.f);
  for (int it = 0; it < 2000000; ++it) {
    u64 t; asm volatile("s_memrealtime %0\n\ts_waitcnt lgkmcnt(0)" : "=s"(t) :: "memory");
    if (t >= end) break;
    __builtin_amdgcn_s_sleep(2);
  }
}
extern "C" void kernel_launch(void* const* d_in, const int* in_sizes, int n_in, void* d_out, int out_size, void* d_ws,
                              size_t ws_size, hipStream_t stream) {
  (void)in_sizes; (void)n_in; (void)out_size; (void)ws_size;
  hipFuncSetAttribute((const void*)fused_kernel, hipFuncAttributeMaxDynamicSharedMemorySize, LDS_BYTES);
  fused_kernel<<<256, 512, LDS_BYTES, stream>>>((const char*)d_in[0], (const char*)d_in[1], (const char*)d_in[2],
                                                (const char*)d_in[3], (const char*)d_in[5], (const char*)d_in[6],
                                                (const char*)d_in[7], (const char*)d_in[8], (char*)d_out, (char*)d_ws);
}
```

```cpp
#include <hip/hip_runtime.h>
#include <stdint.h>

#define EXP_GL 1
typedef unsigned int u32;
typedef unsigned long long u64;
typedef __bf16 bf16x8 __attribute__((ext_vector_type(8)));
typedef __bf16 bf16x2 __attribute__((ext_vector_type(2)));
typedef short v4i16 __attribute__((ext_vector_type(4)));
typedef float f32x4 __attribute__((ext_vector_type(4)));
typedef float f32x2 __attribute__((ext_vector_type(2)));
typedef u32 u32x4 __attribute__((ext_vector_type(4)));
typedef u32 u32x2 __attribute__((ext_vector_type(2)));

#define AS1 __attribute__((address_space(1)))
#define AS3 __attribute__((address_space(3)))
#define AS4 __attribute__((address_space(4)))
#define DEVI __device__ __forceinline__

constexpr u32 WS_EPOCH = 0;
constexpr u32 WS_AFLAG = 1024;
constexpr u32 WS_HFLAG = 2048;
constexpr u32 WS_OFLAG = 4096;
constexpr u32 WS_KVFLAG = 6144;
constexpr u32 WS_STAT = 8192;
constexpr u32 WS_CS = 40960;
constexpr u32 WS_KVP = 0x50000;
constexpr u32 WS_ZP = 0xD0000;
constexpr u32 WS_AF = 0x100000;
constexpr u32 WS_TIM = 0xB00000;
constexpr u32 WS_USED = 0xC00000;

constexpr u32 SLOT = 49152;
constexpr u32 L_SCR = 131072;
constexpr u32 L_ZR = L_SCR + 24576;
constexpr u32 L_CSB = 157696;
constexpr u32 L_MEAN = 159744;
constexpr u32 L_RSTD = 160256;
constexpr u32 L_WPHI = 160768;
constexpr u32 LDS_BYTES = 162816;
constexpr u32 CODE_LINES = 208;

DEVI f32x4 mfma32(u32x4 a, u32x4 b, f32x4 c) {
  return __builtin_amdgcn_mfma_f32_16x16x32_bf16(__builtin_bit_cast(bf16x8, a), __builtin_bit_cast(bf16x8, b), c, 0, 0, 0);
}
DEVI f32x4 mfma16(u32x2 a, u32x2 b, f32x4 c) {
  return __builtin_amdgcn_mfma_f32_16x16x16bf16_1k(__builtin_bit_cast(v4i16, a), __builtin_bit_cast(v4i16, b), c, 0, 0, 0);
}
DEVI u32 pk2(float a, float b) {
  f32x2 v = {a, b};
  return __builtin_bit_cast(u32, __builtin_convertvector(v, bf16x2));
}
DEVI float bflo(u32 v) { return __builtin_bit_cast(float, v << 16); }
DEVI float bfhi(u32 v) { return __builtin_bit_cast(float, v & 0xffff0000u); }
DEVI u32 fbits(float f) { return __builtin_bit_cast(u32, f); }
DEVI float bitsf(u32 u) { return __builtin_bit_cast(float, u); }

DEVI u32x2 lds_tr(AS3 char* L, u32 addr) {
  v4i16 t = __builtin_amdgcn_ds_read_tr16_b64_v4i16((AS3 v4i16*)(L + addr));
  return __builtin_bit_cast(u32x2, t);
}
DEVI u32x4 lds_frag_tr(AS3 char* L, u32 addr, u32 second) {
  u32x2 a = lds_tr(L, addr), b = lds_tr(L, addr + second);
  u32x4 r = {a[0], a[1], b[0], b[1]};
  return r;
}
DEVI u32x4 lds128(AS3 char* L, u32 addr) { return *(AS3 u32x4*)(L + addr); }
DEVI f32x4 lds128f(AS3 char* L, u32 addr) { return *(AS3 f32x4*)(L + addr); }
template <int OFF> DEVI void glds_m0(u32 m0v, const char* sbase, u32 voff) {
  asm volatile("s_mov_b32 m0, %0\n\ts_nop 0\n\tglobal_load_lds_dwordx4 %1, %2 offset:%3" ::"s"(m0v), "v"(voff), "s"(sbase), "n"(OFF) : "memory");
}
template <int OFF> DEVI void glds(const char* sbase, u32 voff) {
  asm volatile("global_load_lds_dwordx4 %0, %1 offset:%2" ::"v"(voff), "s"(sbase), "n"(OFF) : "memory");
}
template <int OFF> DEVI void glds_masked(const char* sbase, u32 voff, u64 mask) {
  asm volatile("s_mov_b64 exec, %3\n\tglobal_load_lds_dwordx4 %0, %1 offset:%2\n\ts_mov_b64 exec, -1" ::"v"(voff), "s"(sbase), "n"(OFF), "s"(mask) : "memory");
}
template <int OFF> DEVI void dsr128(u32x4& d, u32 a) {
  asm volatile("ds_read_b128 %0, %1 offset:%2" : "=v"(d) : "v"(a), "n"(OFF) : "memory");
}
template <int OFF> DEVI void dsrtr(u32x2& d, u32 a) {
  asm volatile("ds_read_b64_tr_b16 %0, %1 offset:%2" : "=v"(d) : "v"(a), "n"(OFF) : "memory");
}
DEVI u32x4 cat(u32x2 a, u32x2 b) { u32x4 r = {a[0], a[1], b[0], b[1]}; return r; }
#define WAIT_V(n) asm volatile("s_waitcnt vmcnt(%0)" ::"n"(n) : "memory")
#define WAIT_L0() asm volatile("s_waitcnt lgkmcnt(0)" ::: "memory")
#define BAR() __builtin_amdgcn_s_barrier()
#define SCHED() __builtin_amdgcn_sched_barrier(0)

DEVI void ld_sc1_x4(u32x4& d, const char* base, u32 off) {
  asm volatile("global_load_dwordx4 %0, %1, %2 sc1" : "=v"(d) : "v"(off), "s"(base) : "memory");
}
DEVI void ld_x4(u32x4& d, const char* base, u32 off) {
  asm volatile("global_load_dwordx4 %0, %1, %2" : "=v"(d) : "v"(off), "s"(base) : "memory");
}
DEVI void ld_x1(u32& d, const char* base, u32 off) {
  asm volatile("global_load_dword %0, %1, %2" : "=v"(d) : "v"(off), "s"(base) : "memory");
}
DEVI void ld_u16(u32& d, const char* base, u32 off) {
  asm volatile("global_load_ushort %0, %1, %2" : "=v"(d) : "v"(off), "s"(base) : "memory");
}
DEVI u32 ld_sc1_x1_wait(const char* base, u32 off) {
  u32 d;
  asm volatile("global_load_dword %0, %1, %2 sc1\n\ts_waitcnt vmcnt(0)" : "=v"(d) : "v"(off), "s"(base) : "memory");
  return d;
}
DEVI void st_sc1_x4(char* base, u32 off, u32x4 v) {
  asm volatile("global_store_dwordx4 %0, %1, %2 sc1\n\ts_nop 1" ::"v"(off), "v"(v), "s"(base) : "memory");
}
DEVI void st_sc1_x2(char* base, u32 off, u32x2 v) {
  asm volatile("global_store_dwordx2 %0, %1, %2 sc1" ::"v"(off), "v"(v), "s"(base) : "memory");
}
DEVI void st_sc1_x1(char* base, u32 off, u32 v) {
  asm volatile("global_store_dword %0, %1, %2 sc1" ::"v"(off), "v"(v), "s"(base) : "memory");
}
constexpr int SPIN_MAX = 1 << 17;
DEVI void poll_flags(const char* base, u32 off0, int n, int l, u32 tag) {
  u32 off = off0 + (u32)(l < n ? l : 0) * 4u;
#pragma unroll 1
  for (int it = 0; it < SPIN_MAX; ++it) {
    u32 v = ld_sc1_x1_wait(base, off);
    if (__builtin_amdgcn_ballot_w64(v != tag) == 0ull) break;
    __builtin_amdgcn_s_sleep(1);
  }
  __builtin_amdgcn_fence(__ATOMIC_ACQUIRE, "wavefront");
}

#define STAMP(i) do {} while (0)
#define STAMPG(i) do {} while (0)
#define STAMP_DECL do {} while (0)
#define STAMP_FLUSH do {} while (0)

DEVI void red4_max2(float& a, float& b) {
  auto s = __builtin_amdgcn_permlane32_swap(fbits(a), fbits(b), false, false);
  float t = fmaxf(bitsf(s[0]), bitsf(s[1]));
  auto u = __builtin_amdgcn_permlane16_swap(fbits(t), fbits(t), false, false);
  float m = fmaxf(bitsf(u[0]), bitsf(u[1]));
  auto z = __builtin_amdgcn_permlane32_swap(fbits(m), fbits(m), false, false);
  a = bitsf(z[0]); b = bitsf(z[1]);
}
DEVI void red4_sum2_rcp(float& a, float& b) {
  auto s = __builtin_amdgcn_permlane32_swap(fbits(a), fbits(b), false, false);
  float t = bitsf(s[0]) + bitsf(s[1]);
  auto u = __builtin_amdgcn_permlane16_swap(fbits(t), fbits(t), false, false);
  float m = __builtin_amdgcn_rcpf(bitsf(u[0]) + bitsf(u[1]));
  auto z = __builtin_amdgcn_permlane32_swap(fbits(m), fbits(m), false, false);
  a = bitsf(z[0]); b = bitsf(z[1]);
}
DEVI u32x4 dual_softmax(f32x4 p) {
  const float L2E = 1.4426950408889634f;
  float mx = fmaxf(fmaxf(p[0], p[1]), fmaxf(p[2], p[3]));
  float nm = fmaxf(fmaxf(-p[0], -p[1]), fmaxf(-p[2], -p[3]));
  red4_max2(mx, nm);
  float a = -mx * L2E, c = -nm * L2E;
  float e0 = __builtin_amdgcn_exp2f(fmaf(p[0], L2E, a)), e1 = __builtin_amdgcn_exp2f(fmaf(p[1], L2E, a));
  float e2 = __builtin_amdgcn_exp2f(fmaf(p[2], L2E, a)), e3 = __builtin_amdgcn_exp2f(fmaf(p[3], L2E, a));
  float n0 = __builtin_amdgcn_exp2f(fmaf(p[0], -L2E, c)), n1 = __builtin_amdgcn_exp2f(fmaf(p[1], -L2E, c));
  float n2 = __builtin_amdgcn_exp2f(fmaf(p[2], -L2E, c)), n3 = __builtin_amdgcn_exp2f(fmaf(p[3], -L2E, c));
  float sp = (e0 + e1) + (e2 + e3), sn = (n0 + n1) + (n2 + n3);
  red4_sum2_rcp(sp, sn);
  u32x4 r = {pk2(e0 * sp, e1 * sp), pk2(e2 * sp, e3 * sp), pk2(n0 * sn, n1 * sn), pk2(n2 * sn, n3 * sn)};
  return r;
}

template <int V> struct IC { static constexpr int value = V; };
template <int N> DEVI void wait4(u32x4& a, u32x4& b, u32x4& c, u32x4& d) {
  asm volatile("s_waitcnt vmcnt(%4)" : "+v"(a), "+v"(b), "+v"(c), "+v"(d) : "n"(N) : "memory");
}
template <int N> DEVI void wait1(u32x4& a) { asm volatile("s_waitcnt vmcnt(%1)" : "+v"(a) : "n"(N) : "memory"); }

template <int N> DEVI void waitset(u32x4& a, u32x4& b, u32x2 (&lo)[8], u32x2 (&hi)[8]);
template <> DEVI void waitset<8>(u32x4& a, u32x4& b, u32x2 (&lo)[8], u32x2 (&hi)[8]) {
  asm volatile("s_waitcnt lgkmcnt(0)"
               : "+v"(a), "+v"(b), "+v"(lo[0]), "+v"(lo[1]), "+v"(lo[2]), "+v"(lo[3]), "+v"(lo[4]), "+v"(lo[5]), "+v"(lo[6]), "+v"(lo[7]),
                 "+v"(hi[0]), "+v"(hi[1]), "+v"(hi[2]), "+v"(hi[3]), "+v"(hi[4]), "+v"(hi[5]), "+v"(hi[6]), "+v"(hi[7])
               :: "memory");
}
template <> DEVI void waitset<6>(u32x4& a, u32x4& b, u32x2 (&lo)[8], u32x2 (&hi)[8]) {
  asm volatile("s_waitcnt lgkmcnt(0)"
               : "+v"(a), "+v"(b), "+v"(lo[0]), "+v"(lo[1]), "+v"(lo[2]), "+v"(lo[3]), "+v"(lo[4]), "+v"(lo[5]),
                 "+v"(hi[0]), "+v"(hi[1]), "+v"(hi[2]), "+v"(hi[3]), "+v"(hi[4]), "+v"(hi[5])
               :: "memory");
}
DEVI void waitb(u32x2 (&lo)[4], u32x2 (&hi)[4]) {
  asm volatile("s_waitcnt lgkmcnt(0)"
               : "+v"(lo[0]), "+v"(lo[1]), "+v"(lo[2]), "+v"(lo[3]), "+v"(hi[0]), "+v"(hi[1]), "+v"(hi[2]), "+v"(hi[3]) :: "memory");
}
DEVI float wave_sum(float v) {
#define DPP_ADD(ctrl, rmask) v += bitsf((u32)__builtin_amdgcn_update_dpp(0, (int)fbits(v), ctrl, rmask, 0xf, false))
  DPP_ADD(0x111, 0xf); DPP_ADD(0x112, 0xf); DPP_ADD(0x114, 0xf); DPP_ADD(0x118, 0xf);
  DPP_ADD(0x142, 0xa); DPP_ADD(0x143, 0xc);
#undef DPP_ADD
  return bitsf((u32)__builtin_amdgcn_readlane((int)fbits(v), 63));
}

extern "C" __global__ void __launch_bounds__(512, 2)
fused_kernel(const char* __restrict__ x, const char* __restrict__ wqkv, const char* __restrict__ wphi,
             const char* __restrict__ wo, const char* __restrict__ w1, const char* __restrict__ b1,
             const char* __restrict__ w2, const char* __restrict__ b2, char* __restrict__ out, char* __restrict__ ws) {
  extern __shared__ __attribute__((aligned(16))) char smem[];
  AS3 char* L = (AS3 char*)smem;
  const int b = blockIdx.x;
  const int l = threadIdx.x & 63;
  const int w = __builtin_amdgcn_readfirstlane(threadIdx.x >> 6);
  u32 pfd;
  {
    u64 pc;
    asm volatile("s_getpc_b64 %0" : "=s"(pc));
    const u32 ln = (u32)(w * 32 + (l & 31));
    ld_x1(pfd, (const char*)(pc & ~127ull), (ln < CODE_LINES ? ln : CODE_LINES - 1u) * 128u);
  }
  asm volatile("" ::"s"(x), "s"(wqkv), "s"(wphi), "s"(wo), "s"(w1), "s"(b1), "s"(w2), "s"(b2), "s"(out), "s"(ws));
  const int xcd = b & 7, bi = b >> 3;
  const int p = (xcd >> 2) * 8 + (bi >> 2), c = (xcd & 3) * 4 + (bi & 3), g = c >> 1;
  const bool typeA = (c & 1) == 0;
  const int rg = w & 3, cg = w >> 2;
  const int gq = l >> 4, r16 = l & 15, q4 = (l >> 2) & 3, pp = l & 3;
  const int rot = (p + c) & 7;
  const int rb = 4 * xcd + (bi >> 3), cb = bi & 7;
  STAMP_DECL;
  STAMP(0);

  const u32 lb = (u32)(size_t)L;
  const u32 xrow_off = (u32)(128 * p + 8 * c + w) * 1024u + (u32)l * 16u;
  u32 gb1[2];
  const u32 gb1_off = (u32)(224 * g + (w < 6 ? 32 * w + ((g & 1) ? 32 : 0) : (w == 6 && !(g & 1) ? 192 : 0)) + (l >> 2)) * 2u;
  u32 tag;

  const char* xsrc = x + (size_t)p * 131072u;
  const u32 wstride = typeA ? 3072u : 3584u;
  const char* wsrc = (typeA ? (wqkv + g * 128) : (w1 + 448 * g)) - 4096;
  const u32 vx0 = (u32)(16 * w + (l >> 3)) * 1024u + (u32)(((l & 7) ^ (l >> 4)) * 16);
  const u32 vx1 = (u32)(16 * w + 7 + (l >> 3)) * 1024u + (u32)(((l & 7) ^ (4 + (l >> 4))) * 16);
  const u32 wsp = (u32)(((l & 7) >> 1) ^ (((l >> 4) & 1) | ((w & 1) << 1)));
  const u32 vw = (u32)(8 * w + (l >> 3)) * wstride + (wsp * 2u + (u32)(l & 1)) * 16u + 4096u;
  u32 vws[4];
  {
    const u32 odd = (u32)(g & 1) * 64u;
    vws[0] = typeA ? vw : vw + odd;
    vws[1] = typeA ? vw : vw + odd + 128u - 1024u;
    vws[2] = typeA ? vw : vw + odd + 256u - 2048u;
    vws[3] = vw + (odd ? 0u : 384u) - 3072u;
  }
  const u64 hmask = __builtin_amdgcn_ballot_w64(wsp < 2u);
  const u32 m0x = lb + (u32)w * 2048u, m0w = lb + 16384u + (u32)w * 4096u;

  const char* dsrc2 = w2 + (size_t)(7 * w) * 32768u + cb * 128;
  const char* dsrco = wo + (size_t)(2 * w) * 32768u + cb * 128;
  const u32 vdb = (u32)(l >> 3) * 1024u + (u32)(((((l & 7) >> 1) ^ ((l >> 4) & 3)) * 2 + (l & 1)) * 16);
  auto dslot = [&](int j) -> u32 { return (u32)((j + 3) & 3) * 32768u + (u32)w * 4096u; };
  auto issueB = [&](int j) {
    const char* s = (j < 7) ? (dsrc2 + j * 32768) : (dsrco + (j - 7) * 32768);
    glds_m0<0>(lb + dslot(j), s, vdb);
    glds<1024>(s, vdb + 7168u);
    glds<2048>(s, vdb + 2u * 7168u);
    glds<3072>(s, vdb + 3u * 7168u);
  };

  u32 xa0 = lb + (u32)(32 * rg + r16) * 128u + (u32)((gq ^ ((r16 >> 1) & 7)) * 16);
  u32 xa1 = xa0 ^ 64u;
  const u32 vbase = lb + 16384u + (u32)gq * 4096u + (u32)q4 * 128u + (u32)pp * 8u + (u32)(((q4 >> 1) & 1) | ((gq & 1) << 1)) * 32u;
  u32 R[8];
#pragma unroll
  for (int t = 0; t < 8; ++t) {
    const u32 ra = (vbase ^ (u32)((2 * cg + (t & 1)) * 32)) + (u32)(t >> 1) * 1024u;
    const u32 rbb = (vbase ^ (u32)((t & 3) * 32)) + (u32)(t >> 2) * 1024u + (u32)cg * 2048u;
    R[t] = typeA ? ra : rbb;
  }

  f32x4 acc[8][2];
#pragma unroll
  for (int t = 0; t < 8; ++t) { acc[t][0] = f32x4{0, 0, 0, 0}; acc[t][1] = f32x4{0, 0, 0, 0}; }
  u32x4 gat[8];
  const u32x4 ones = {0x3F803F80u, 0x3F803F80u, 0x3F803F80u, 0x3F803F80u};
  const u32 gcs = WS_CS + (u32)(c * 256 + 32 * w + (l >> 2)) * 64u + (u32)(l & 3) * 16u;
  const u32 gst = WS_STAT + (u32)(128 * p + 16 * w + r16) * 16u;

  auto g1 = [&](auto TAc, auto N8c) {
    constexpr bool TA = decltype(TAc)::value != 0, N8 = decltype(N8c)::value != 0;
    constexpr int NG = TA ? 5 : 6;
    u32x4 fx[2][2];
    u32x2 flo[2][8], fhi[2][8];
    u32x4 xrow;
    u32 epoch;
    asm volatile("s_load_dword %0, %1, 0x0" : "=s"(epoch) : "s"(ws) : "memory");
    ld_x4(xrow, x, xrow_off);
    if (TA) { gb1[0] = 0; gb1[1] = 0; }
    else { ld_u16(gb1[0], b1, gb1_off); ld_u16(gb1[1], b1, gb1_off + 32u); }
    auto issue = [&](int s, u32 sb) {
      const int ks = (s + rot) & 7;
      const char* xs = xsrc + ks * 128;
      const char* wsb = wsrc + (size_t)((u32)ks * 64u * wstride);
      glds_m0<0>(m0x + sb, xs, vx0);
      glds<1024>(xs, vx1);
      glds_m0<0>(m0w + sb, wsb, vws[0]);
      glds<1024>(wsb, vws[1]);
      glds<2048>(wsb, vws[2]);
      if (!TA) glds_masked<3072>(wsb, vws[3], hmask);
    };
    issue(0, 0); issue(1, SLOT); issue(2, 2 * SLOT);
    if (TA && w < 2) glds_m0<0>(lb + L_WPHI + (u32)w * 1024u, wphi + g * 2048 + w * 1024, (u32)l * 16u);

#define MF(S, t, rt) acc[t][rt] = mfma32(cat(flo[S][t], fhi[S][t]), fx[S][rt], acc[t][rt])
#define MFS(S, t, rt) acc[t][rt] = mfma32(fx[S][rt], cat(flo[S][t], fhi[S][t]), acc[t][rt])
#define RW(N, t) do { if (RD) { dsrtr<(N) * 16384>(flo[N][t], R[t]); dsrtr<(N) * 16384 + 512>(fhi[N][t], R[t]); } } while (0)
#define RX(N) do { if (RD) { dsr128<0>(fx[N][0], (N) ? xa1 : xa0); dsr128<2048>(fx[N][1], (N) ? xa1 : xa0); } } while (0)
    auto waitS = [&](auto Sc) {
      constexpr int S = decltype(Sc)::value;
      if constexpr (N8) waitset<8>(fx[S][0], fx[S][1], flo[S], fhi[S]);
      else waitset<6>(fx[S][0], fx[S][1], flo[S], fhi[S]);
    };
    auto phase = [&](auto Sc, auto RDc, auto GLc, const char* xs, const char* wsb, u32 sb) {
      constexpr int S = decltype(Sc)::value, N = 1 - S;
      constexpr bool RD = decltype(RDc)::value != 0, GL = decltype(GLc)::value != 0;
      waitS(Sc);
      if (S == 1 && RD) BAR();
      SCHED();
      if constexpr (N8) {
        MF(S, 6, 0); RW(N, 6); SCHED();
        MF(S, 6, 1); RW(N, 7); SCHED();
        MF(S, 7, 0); SCHED();
        MF(S, 7, 1); SCHED();
      }
      if constexpr (TA) {
        MFS(S, 4, 0); RX(N); SCHED();
        MFS(S, 4, 1); RW(N, 4); SCHED();
        MFS(S, 5, 0); RW(N, 5); SCHED();
        MFS(S, 5, 1); RW(N, 0); SCHED();
      } else {
        MF(S, 4, 0); RX(N); SCHED();
        MF(S, 4, 1); RW(N, 4); SCHED();
        MF(S, 5, 0); RW(N, 5); SCHED();
        MF(S, 5, 1); RW(N, 0); SCHED();
      }
      MF(S, 0, 0); RW(N, 1); SCHED();
      MF(S, 0, 1); RW(N, 2); SCHED();
      MF(S, 1, 0); RW(N, 3); SCHED();
      MF(S, 1, 1); if (GL) { glds_m0<0>(m0x + sb, xs, vx0); glds<1024>(xs, vx1); } SCHED();
      MF(S, 2, 0); if (GL) glds_m0<0>(m0w + sb, wsb, vws[0]); SCHED();
      MF(S, 2, 1); if (GL) glds<1024>(wsb, vws[1]); SCHED();
      MF(S, 3, 0); if (GL) glds<2048>(wsb, vws[2]); SCHED();
      MF(S, 3, 1); if (GL && !TA) glds_masked<3072>(wsb, vws[3], hmask); SCHED();
    };
    auto advance = [&](u32 d) {
#pragma unroll
      for (int t = 0; t < (N8 ? 8 : 6); ++t) R[t] += d;
      xa0 += d; xa1 += d;
    };

    STAMP(1);
    if (TA) asm volatile("s_waitcnt vmcnt(%1)" : "+v"(xrow) : "n"(2 * NG) : "memory");
    else asm volatile("s_waitcnt vmcnt(%3)" : "+v"(xrow), "+v"(gb1[0]), "+v"(gb1[1]) : "n"(2 * NG) : "memory");
    asm volatile("" ::"v"(pfd));
    STAMPG(1);
    BAR();
    STAMPG(2);
    {
      constexpr bool RD = true;
      RX(0); RW(0, 4); RW(0, 5); RW(0, 0); RW(0, 1); RW(0, 2); RW(0, 3);
      if constexpr (N8) { RW(0, 6); RW(0, 7); }
    }
    waitS(IC<0>{});
    asm volatile("" : "+s"(epoch) :: "memory");
    tag = epoch + 1u;
    phase(IC<0>{}, IC<1>{}, IC<0>{}, nullptr, nullptr, 0u);
    {
      float v[8];
#pragma unroll
      for (int i = 0; i < 4; ++i) { v[2 * i] = bflo(xrow[i]); v[2 * i + 1] = bfhi(xrow[i]); }
      const float mean = wave_sum(((v[0] + v[1]) + (v[2] + v[3])) + ((v[4] + v[5]) + (v[6] + v[7]))) * (1.0f / 512.0f);
      float qq = 0.f;
#pragma unroll
      for (int i = 0; i < 8; ++i) { float d = v[i] - mean; qq = fmaf(d, d, qq); }
      const float rstd = 1.0f / sqrtf(wave_sum(qq) * (1.0f / 512.0f) + 1e-5f);
      if (l == 0) {
        u32x4 gr = {fbits(mean), fbits(rstd), tag, tag};
        st_sc1_x4(ws, WS_STAT + (u32)(128 * p + 8 * c + w) * 16u, gr);
        if (w == 7) st_sc1_x1(ws, WS_AFLAG + (u32)b * 4u, tag);
      }
      if (p < 8 && w < (TA ? 6 : 7)) {
        u32x2 c0l[2], c0h[2], c1l[2], c1h[2];
        const u32 a0 = (vbase ^ (u32)((2 * (w & 1)) * 32)) + (u32)(w >> 1) * 1024u;
        const u32 a1 = a0 ^ 32u;
        dsrtr<0>(c0l[0], a0); dsrtr<512>(c0h[0], a0); dsrtr<16384>(c0l[1], a0); dsrtr<16384 + 512>(c0h[1], a0);
        dsrtr<0>(c1l[0], a1); dsrtr<512>(c1h[0], a1); dsrtr<16384>(c1l[1], a1); dsrtr<16384 + 512>(c1h[1], a1);
        asm volatile("s_waitcnt lgkmcnt(0)" : "+v"(c0l[0]), "+v"(c0h[0]), "+v"(c0l[1]), "+v"(c0h[1]),
                     "+v"(c1l[0]), "+v"(c1h[0]), "+v"(c1l[1]), "+v"(c1h[1]) :: "memory");
        f32x4 c0 = {0, 0, 0, 0}, c1 = {0, 0, 0, 0};
#pragma unroll
        for (int ks = 0; ks < 2; ++ks) {
          c0 = mfma32(ones, cat(c0l[ks], c0h[ks]), c0);
          c1 = mfma32(ones, cat(c1l[ks], c1h[ks]), c1);
        }
        if (l < 32) {
          u32x2 gr = {fbits(l < 16 ? c0[0] : c1[0]), tag};
          st_sc1_x2(ws, WS_CS + (u32)((c * 256 + 32 * w + l) * 8 + rot) * 8u, gr);
        }
      }
    }
    u32 sb = 0;
#pragma unroll 1
    for (int s = 0; s < 5; ++s) {
      const u32 nsb = sb == 2 * SLOT ? 0u : sb + SLOT;
      advance(nsb - sb);
      const int ks = (s + 3 + rot) & 7;
      WAIT_V(NG);
      STAMPG(3 + s);
      phase(IC<1>{}, IC<1>{}, IC<EXP_GL>{}, xsrc + ks * 128, wsrc + (size_t)((u32)ks * 64u * wstride), sb);
      phase(IC<0>{}, IC<1>{}, IC<0>{}, nullptr, nullptr, 0u);
      sb = nsb;
    }
    advance(0u - 2u * SLOT);
    WAIT_V(NG);
    STAMPG(8);
    phase(IC<1>{}, IC<1>{}, IC<0>{}, nullptr, nullptr, 0u);
    ld_sc1_x4(gat[0], ws, gcs);
    ld_sc1_x4(gat[1], ws, gcs + 1024u);
    ld_sc1_x4(gat[2], ws, gst);
    issueB(0);
    phase(IC<0>{}, IC<1>{}, IC<0>{}, nullptr, nullptr, 0u);
    advance(SLOT);
    WAIT_V(7);
    STAMPG(9);
    phase(IC<1>{}, IC<1>{}, IC<0>{}, nullptr, nullptr, 0u);
    issueB(1);
    phase(IC<0>{}, IC<1>{}, IC<0>{}, nullptr, nullptr, 0u);
    phase(IC<1>{}, IC<0>{}, IC<0>{}, nullptr, nullptr, 0u);
#undef MF
#undef MFS
#undef RW
#undef RX
    STAMP(2);
    STAMPG(10);
    asm volatile("s_waitcnt vmcnt(8)" : "+v"(gat[0]), "+v"(gat[1]), "+v"(gat[2]) :: "memory");
    STAMPG(11);
  };
  if (typeA) g1(IC<1>{}, IC<0>{});
  else if (cg == 0) g1(IC<0>{}, IC<1>{});
  else g1(IC<0>{}, IC<0>{});
  {
    const bool used = w < (typeA ? 6 : 7);
  #pragma unroll 1
  for (int it = 0; it < SPIN_MAX; ++it) {
      const bool bad = (used && ((gat[0][1] != tag) | (gat[0][3] != tag) | (gat[1][1] != tag) | (gat[1][3] != tag))) | (gat[2][2] != tag);
      if (__builtin_amdgcn_ballot_w64(bad) == 0ull) break;
      ld_sc1_x4(gat[0], ws, gcs);
      ld_sc1_x4(gat[1], ws, gcs + 1024u);
      ld_sc1_x4(gat[2], ws, gst);
      asm volatile("s_waitcnt vmcnt(0)" : "+v"(gat[0]), "+v"(gat[1]), "+v"(gat[2]) :: "memory");
    }
#pragma unroll
    for (int i = 0; i < 2; ++i) {
      float sm = bitsf(gat[i][0]) + bitsf(gat[i][2]);
      sm += bitsf((u32)__builtin_amdgcn_update_dpp(0, (int)fbits(sm), 0xB1, 0xf, 0xf, false));
      sm += bitsf((u32)__builtin_amdgcn_update_dpp(0, (int)fbits(sm), 0x4E, 0xf, 0xf, false));
      if ((l & 3) == 0) {
        f32x2 o = {sm, bflo(gb1[i])};
        *(AS3 f32x2*)(L + L_CSB + (u32)(32 * w + 16 * i + (l >> 2)) * 8u) = o;
      }
    }
    if (l < 16) {
      *(AS3 float*)(L + L_MEAN + (u32)(16 * w + l) * 4u) = bitsf(gat[2][0]);
      *(AS3 float*)(L + L_RSTD + (u32)(16 * w + l) * 4u) = bitsf(gat[2][1]);
    }
  }
  WAIT_L0();
  BAR();
  issueB(2); issueB(3);
  STAMP(3);

  const float mean0 = *(AS3 float*)(L + L_MEAN + (u32)(32 * rg + r16) * 4u);
  const float mean1 = *(AS3 float*)(L + L_MEAN + (u32)(32 * rg + 16 + r16) * 4u);
  const float rstd0 = *(AS3 float*)(L + L_RSTD + (u32)(32 * rg + r16) * 4u);
  const float rstd1 = *(AS3 float*)(L + L_RSTD + (u32)(32 * rg + 16 + r16) * 4u);

  const u32 vb2 = (u32)(4 * gq + q4) * 128u + (u32)pp * 8u + (u32)((2 * gq + (q4 >> 1)) & 3) * 32u;
  u32x2 blo[4][4], bhi[4][4];
  u32 VB[4];
#pragma unroll
  for (int ct = 0; ct < 4; ++ct) VB[ct] = lb + (vb2 ^ (u32)(ct * 32)) + (u32)w * 4096u;
  auto rdB = [&](auto jc) {
    constexpr int j = decltype(jc)::value, blk = (j + 3) & 3;
#pragma unroll
    for (int ct = 0; ct < 4; ++ct) {
      const u32 a = VB[ct] + (blk >= 2 ? 65536u : 0u);
      dsrtr<(blk & 1) * 32768>(blo[j & 3][ct], a);
      dsrtr<(blk & 1) * 32768 + 2048>(bhi[j & 3][ct], a);
    }
  };
  auto loadBreg = [&]() {
    rdB(IC<0>{}); rdB(IC<1>{}); waitb(blo[0], bhi[0]); waitb(blo[1], bhi[1]);
    rdB(IC<2>{}); rdB(IC<3>{}); waitb(blo[2], bhi[2]); waitb(blo[3], bhi[3]);
    issueB(4); issueB(5); issueB(6); issueB(7);
  };

  if (!typeA) {
    auto hpair = [&](auto Pc) {
      constexpr int P = decltype(Pc)::value;
      const int hl = cg * 4 + P;
      u32x4 o0, o1;
#pragma unroll
      for (int e = 0; e < 2; ++e) {
        const int t = 2 * P + e;
        const u32 ca = L_CSB + (u32)(hl * 32 + e * 16 + 4 * gq) * 8u;
        const f32x4 cb0 = lds128f(L, ca), cb1 = lds128f(L, ca + 16u);
        const float cs[4] = {cb0[0], cb0[2], cb1[0], cb1[2]}, bb[4] = {cb0[1], cb0[3], cb1[1], cb1[3]};
        float h0[4], h1[4];
#pragma unroll
        for (int r = 0; r < 4; ++r) {
          float y0 = fmaf(rstd0, fmaf(-mean0, cs[r], acc[t][0][r]), bb[r]);
          float y1 = fmaf(rstd1, fmaf(-mean1, cs[r], acc[t][1][r]), bb[r]);
          y0 = fmaxf(y0, 0.f); y1 = fmaxf(y1, 0.f);
          h0[r] = y0 * y0; h1[r] = y1 * y1;
        }
        o0[2 * e] = pk2(h0[0], h0[1]); o0[2 * e + 1] = pk2(h0[2], h0[3]);
        o1[2 * e] = pk2(h1[0], h1[1]); o1[2 * e + 1] = pk2(h1[2], h1[3]);
      }
      const u32 step = (u32)(7 * g + ((g & 1) ? (hl < 6 ? hl + 1 : 0) : hl));
      const u32 off = WS_AF + ((u32)(8 * p + 2 * rg) * 72u + step) * 1024u + (u32)l * 16u;
      st_sc1_x4(ws, off, o0);
      st_sc1_x4(ws, off + 72u * 1024u, o1);
    };
    hpair(IC<0>{}); hpair(IC<1>{}); hpair(IC<2>{});
    if (cg == 0) hpair(IC<3>{});
    WAIT_V(0);
    BAR();
    if (threadIdx.x == 0) st_sc1_x1(ws, WS_HFLAG + (u32)p * 128u + (u32)g * 4u, tag);
    STAMP(4);
    loadBreg();
  } else {
    const u32x2 ident = {(4 * gq + 0 == r16 ? 0x3F80u : 0u) | (4 * gq + 1 == r16 ? 0x3F800000u : 0u),
                         (4 * gq + 2 == r16 ? 0x3F80u : 0u) | (4 * gq + 3 == r16 ? 0x3F800000u : 0u)};
    const f32x4 zero4 = {0, 0, 0, 0};
    const f32x4 vm0 = lds128f(L, L_MEAN + (u32)(32 * rg + 4 * gq) * 4u), vm1 = lds128f(L, L_MEAN + (u32)(32 * rg + 16 + 4 * gq) * 4u);
    const f32x4 vr0 = lds128f(L, L_RSTD + (u32)(32 * rg + 4 * gq) * 4u), vr1 = lds128f(L, L_RSTD + (u32)(32 * rg + 16 + 4 * gq) * 4u);
    u32x2 wp[2];
    auto logits = [&](int line, int pi, int rt) -> f32x4 {
      const int t = line * 2 + pi;
      const u32 ca = L_CSB + (u32)(line * 64 + (2 * cg + pi) * 16 + 4 * gq) * 8u;
      const f32x4 cb0 = lds128f(L, ca), cb1 = lds128f(L, ca + 16u);
      const float mn = rt ? mean1 : mean0, rs = rt ? rstd1 : rstd0;
      const float y0 = rs * fmaf(-mn, cb0[0], acc[t][rt][0]), y1 = rs * fmaf(-mn, cb0[2], acc[t][rt][1]);
      const float y2 = rs * fmaf(-mn, cb1[0], acc[t][rt][2]), y3 = rs * fmaf(-mn, cb1[2], acc[t][rt][3]);
      const u32x2 kb = {pk2(y0, y1), pk2(y2, y3)};
      return mfma16(wp[pi], kb, zero4);
    };
#pragma unroll
    for (int pi = 0; pi < 2; ++pi)
      wp[pi] = lds_tr(L, L_WPHI + (u32)((2 * cg + pi) * 512 + (4 * gq + q4) * 32 + pp * 8));
    f32x4 kvt[4], zt[4];
#pragma unroll
    for (int pi = 0; pi < 2; ++pi) {
      u32x4 tp, tn, vv;
#pragma unroll
      for (int rt = 0; rt < 2; ++rt) {
        const u32x4 ph = dual_softmax(logits(1, pi, rt));
        const f32x4 a = mfma16(u32x2{ph[0], ph[1]}, ident, zero4);
        const f32x4 n = mfma16(u32x2{ph[2], ph[3]}, ident, zero4);
        tp[2 * rt] = pk2(a[0], a[1]); tp[2 * rt + 1] = pk2(a[2], a[3]);
        tn[2 * rt] = pk2(n[0], n[1]); tn[2 * rt + 1] = pk2(n[2], n[3]);
        const float cs = *(AS3 float*)(L + L_CSB + (u32)(128 + (2 * cg + pi) * 16 + r16) * 8u);
        const f32x4 vm = rt ? vm1 : vm0, vr = rt ? vr1 : vr0;
        const f32x4 av = acc[4 + pi][rt];
        vv[2 * rt] = pk2(vr[0] * fmaf(-vm[0], cs, av[0]), vr[1] * fmaf(-vm[1], cs, av[1]));
        vv[2 * rt + 1] = pk2(vr[2] * fmaf(-vm[2], cs, av[2]), vr[3] * fmaf(-vm[3], cs, av[3]));
      }
      kvt[2 * pi] = mfma32(tp, vv, zero4); kvt[2 * pi + 1] = mfma32(tn, vv, zero4);
      zt[2 * pi] = mfma32(tp, ones, zero4); zt[2 * pi + 1] = mfma32(tn, ones, zero4);
    }
    {
      const int sl = rg > 0 ? rg - 1 : 0;
#pragma unroll
      for (int pi = 0; pi < 2; ++pi) {
        if (rg != pi) {
#pragma unroll
          for (int sg = 0; sg < 2; ++sg)
            *(AS3 f32x4*)(L + L_SCR + (u32)((((cg * 2 + pi) * 2 + sg) * 3 + sl) * 1024 + l * 16)) = kvt[2 * pi + sg];
        }
        if (r16 == 0) {
#pragma unroll
          for (int sg = 0; sg < 2; ++sg)
            *(AS3 f32x4*)(L + L_ZR + (u32)(((((cg * 2 + pi) * 2 + sg) * 4 + rg) * 16 + 4 * gq) * 4)) = zt[2 * pi + sg];
        }
      }
    }
    WAIT_L0();
    BAR();
    if (rg < 2) {
      f32x4 sp = rg == 0 ? kvt[0] : kvt[2], sn = rg == 0 ? kvt[1] : kvt[3];
#pragma unroll
      for (int sl = 0; sl < 3; ++sl) {
        sp += lds128f(L, L_SCR + (u32)((((cg * 2 + rg) * 2 + 0) * 3 + sl) * 1024 + l * 16));
        sn += lds128f(L, L_SCR + (u32)((((cg * 2 + rg) * 2 + 1) * 3 + sl) * 1024 + l * 16));
      }
      u32x4 o = {pk2(sp[0], sp[1]), pk2(sp[2], sp[3]), pk2(sn[0], sn[1]), pk2(sn[2], sn[3])};
      st_sc1_x4(ws, WS_KVP + (u32)(((g * 16 + p) * 4 + 2 * cg + rg) * 1024 + l * 16), o);
    } else if (l < 8) {
      const int pi = rg - 2, sg = l >> 2, qq = l & 3;
      f32x4 s = {0, 0, 0, 0};
#pragma unroll
      for (int r = 0; r < 4; ++r) s += lds128f(L, L_ZR + (u32)(((((cg * 2 + pi) * 2 + sg) * 4 + r) * 16 + 4 * qq) * 4));
      st_sc1_x4(ws, WS_ZP + (u32)((((g * 4 + 2 * cg + pi) * 16 + p) * 32 + l * 4) * 4), __builtin_bit_cast(u32x4, s));
    }
    WAIT_V(0);
    BAR();
    if (threadIdx.x == 0) st_sc1_x1(ws, WS_KVFLAG + (u32)g * 128u + (u32)p * 4u, tag);
    STAMP(4);
    loadBreg();
    u32x4 phq[2][2];
#pragma unroll
    for (int pi = 0; pi < 2; ++pi)
#pragma unroll
      for (int rt = 0; rt < 2; ++rt) phq[pi][rt] = dual_softmax(logits(0, pi, rt));
    STAMP(5);
    if (w == 0) poll_flags(ws, WS_KVFLAG + (u32)g * 128u, 16, l, tag);
    BAR();
    STAMP(6);
    {
      const int hd = w & 3, hf = w >> 2;
#pragma unroll
      for (int i = 0; i < 8; ++i) ld_sc1_x4(gat[i], ws, WS_KVP + (u32)(((g * 16 + 8 * hf + i) * 4 + hd) * 1024 + l * 16));
      u32x4 zg;
      ld_sc1_x4(zg, ws, WS_ZP + (u32)(((g * 4 + hd) * 16 + 8 * hf) * 128 + l * 16));
      asm volatile("s_waitcnt vmcnt(0)" : "+v"(gat[0]), "+v"(gat[1]), "+v"(gat[2]), "+v"(gat[3]), "+v"(gat[4]), "+v"(gat[5]),
                   "+v"(gat[6]), "+v"(gat[7]), "+v"(zg) :: "memory");
      f32x4 s0 = {0, 0, 0, 0}, s1 = {0, 0, 0, 0};
#pragma unroll
      for (int i = 0; i < 8; ++i) {
        s0[0] += bflo(gat[i][0]); s0[1] += bfhi(gat[i][0]); s0[2] += bflo(gat[i][1]); s0[3] += bfhi(gat[i][1]);
        s1[0] += bflo(gat[i][2]); s1[1] += bfhi(gat[i][2]); s1[2] += bflo(gat[i][3]); s1[3] += bfhi(gat[i][3]);
      }
      *(AS3 f32x4*)(L + L_SCR + (u32)((hd * 2 + hf) * 2048 + l * 32)) = s0;
      *(AS3 f32x4*)(L + L_SCR + (u32)((hd * 2 + hf) * 2048 + l * 32 + 16)) = s1;
      f32x4 zs = __builtin_bit_cast(f32x4, zg);
#pragma unroll
      for (int r = 0; r < 4; ++r) {
        float v = zs[r];
        v += __shfl_xor(v, 8); v += __shfl_xor(v, 16); v += __shfl_xor(v, 32);
        zs[r] = v;
      }
      if (l < 8) *(AS3 f32x4*)(L + L_ZR + (u32)(((hd * 2 + hf) * 32 + l * 4) * 4)) = zs;
    }
    WAIT_L0();
    BAR();
    {
      u32x4 o0, o1;
#pragma unroll
      for (int pi = 0; pi < 2; ++pi) {
        const int hd = 2 * cg + pi;
        const u32 ga = L_SCR + (u32)(hd * 4096 + l * 32);
        const f32x4 kp = lds128f(L, ga) + lds128f(L, ga + 2048u), kn = lds128f(L, ga + 16u) + lds128f(L, ga + 2064u);
        const u32 za = L_ZR + (u32)((hd * 64 + 4 * gq) * 4);
        const f32x4 zp = lds128f(L, za) + lds128f(L, za + 128u), zn = lds128f(L, za + 64u) + lds128f(L, za + 192u);
        const u32x4 kf = {pk2(kp[0], kp[1]), pk2(kp[2], kp[3]), pk2(kn[0], kn[1]), pk2(kn[2], kn[3])};
        const u32x4 zf = {pk2(zp[0], zp[1]), pk2(zp[2], zp[3]), pk2(zn[0], zn[1]), pk2(zn[2], zn[3])};
#pragma unroll
        for (int rt = 0; rt < 2; ++rt) {
          const f32x4 num = mfma32(kf, phq[pi][rt], zero4), den = mfma32(zf, phq[pi][rt], zero4);
          const float rd = __builtin_amdgcn_rcpf(den[0] + 1e-6f);
          const u32 a = pk2(num[0] * rd, num[1] * rd), bq = pk2(num[2] * rd, num[3] * rd);
          if (rt == 0) { o0[2 * pi] = a; o0[2 * pi + 1] = bq; } else { o1[2 * pi] = a; o1[2 * pi + 1] = bq; }
        }
      }
      const u32 off = WS_AF + ((u32)(8 * p + 2 * rg) * 72u + (u32)(56 + 2 * g + cg)) * 1024u + (u32)l * 16u;
      st_sc1_x4(ws, off, o0);
      st_sc1_x4(ws, off + 72u * 1024u, o1);
    }
    WAIT_V(0);
    BAR();
    if (threadIdx.x == 0) st_sc1_x1(ws, WS_OFLAG + (u32)p * 128u + (u32)g * 4u, tag);
    STAMP(7);
  }

  {
    const int dp = rb >> 1;
    u32x4 xres, b2v;
    const u32 fo = (u32)(64 * rb + (threadIdx.x >> 3)) * 1024u + (u32)(cb * 128 + (threadIdx.x & 7) * 16);
    if (w == 0) poll_flags(ws, WS_HFLAG + (u32)dp * 128u, 8, l, tag);
    BAR();
    STAMP(8);
    const char* af = ws + WS_AF + (size_t)(4 * rb) * 73728u;
    const char* afh = af + (size_t)(7 * w) * 1024u;
    const char* afo = af + (size_t)(56 + 2 * w) * 1024u;
    const u32 vo = (u32)l * 16u;
    f32x4 dacc[4][4];
#pragma unroll
    for (int i = 0; i < 4; ++i)
#pragma unroll
      for (int j = 0; j < 4; ++j) dacc[i][j] = f32x4{0, 0, 0, 0};
    u32x4 a[9][4];
    auto loadA = [&](int j) {
      const char* s = j < 7 ? afh + j * 1024 : afo + (j - 7) * 1024;
#pragma unroll
      for (int rt = 0; rt < 4; ++rt) ld_sc1_x4(a[j][rt], s + rt * 73728, vo);
    };
    auto waitA = [&](int j, auto n) { wait4<decltype(n)::value>(a[j][0], a[j][1], a[j][2], a[j][3]); };
    auto step = [&](int j) {
#pragma unroll
      for (int ct = 0; ct < 4; ++ct) {
        const u32x4 bf = cat(blo[j & 3][ct], bhi[j & 3][ct]);
#pragma unroll
        for (int rt = 0; rt < 4; ++rt) dacc[rt][ct] = mfma32(bf, a[j][rt], dacc[rt][ct]);
      }
      SCHED();
    };
    loadA(0); loadA(1); loadA(2); loadA(3);
    ld_x4(xres, x, fo);
    waitA(0, IC<13>{}); step(0); rdB(IC<4>{}); loadA(4);
    waitA(1, IC<13>{}); waitb(blo[0], bhi[0]); issueB(8); step(1); rdB(IC<5>{}); loadA(5);
    waitA(2, IC<17>{}); step(2); rdB(IC<6>{}); loadA(6);
    waitA(3, IC<17>{}); step(3); rdB(IC<7>{});
    waitA(4, IC<12>{}); step(4);
    waitA(5, IC<4>{}); waitb(blo[1], bhi[1]); step(5); rdB(IC<8>{});
    waitA(6, IC<0>{}); waitb(blo[2], bhi[2]); step(6);
    STAMP(9);
    poll_flags(ws, WS_OFLAG + (u32)dp * 128u + (u32)w * 4u, 1, l, tag);
    STAMP(10);
    loadA(7); loadA(8);
    ld_x4(b2v, b2, (u32)(cb * 128 + (threadIdx.x & 7) * 16));
    waitA(7, IC<5>{}); waitb(blo[3], bhi[3]); step(7);
    waitA(8, IC<1>{}); waitb(blo[0], bhi[0]); step(8);
    WAIT_L0();
#pragma unroll
    for (int rt = 0; rt < 4; ++rt)
#pragma unroll
      for (int ct = 0; ct < 4; ++ct)
        *(AS3 f32x4*)(L + dslot(rt) + (u32)(r16 * 256 + (((ct * 4 + gq) ^ r16) * 16))) = dacc[rt][ct];
    WAIT_L0();
    BAR();
    STAMP(11);
    {
      const int row = threadIdx.x >> 3, cq = threadIdx.x & 7, rt = row >> 4, tk = row & 15;
      const u32 pb = (u32)((rt + 3) & 3) * 32768u + (u32)(tk * 256);
      f32x4 s0 = {0, 0, 0, 0}, s1 = {0, 0, 0, 0};
#pragma unroll
      for (int ww = 0; ww < 8; ++ww) {
        s0 += lds128f(L, pb + (u32)(ww * 4096) + (u32)(((2 * cq) ^ tk) * 16));
        s1 += lds128f(L, pb + (u32)(ww * 4096) + (u32)(((2 * cq + 1) ^ tk) * 16));
      }
      asm volatile("s_waitcnt vmcnt(0)" : "+v"(xres), "+v"(b2v) :: "memory");
      u32x4 o;
      o[0] = pk2(s0[0] + bflo(xres[0]) + bflo(b2v[0]), s0[1] + bfhi(xres[0]) + bfhi(b2v[0]));
      o[1] = pk2(s0[2] + bflo(xres[1]) + bflo(b2v[1]), s0[3] + bfhi(xres[1]) + bfhi(b2v[1]));
      o[2] = pk2(s1[0] + bflo(xres[2]) + bflo(b2v[2]), s1[1] + bfhi(xres[2]) + bfhi(b2v[2]));
      o[3] = pk2(s1[2] + bflo(xres[3]) + bflo(b2v[3]), s1[3] + bfhi(xres[3]) + bfhi(b2v[3]));
      *(u32x4*)(out + fo) = o;
    }
    if (b == 255 && w == 7) {
#pragma unroll 1
      for (int i = 0; i < 4; ++i) poll_flags(ws, WS_AFLAG + (u32)i * 256u, 64, l, tag);
      if (l == 0) st_sc1_x1(ws, WS_EPOCH, tag);
    }
    STAMP(12);
    STAMP_FLUSH;
  }
}

extern "C" void kernel_launch(void* const* d_in, const int* in_sizes, int n_in, void* d_out, int out_size, void* d_ws,
                              size_t ws_size, hipStream_t stream) {
  (void)in_sizes; (void)n_in; (void)out_size; (void)ws_size;
  hipFuncSetAttribute((const void*)fused_kernel, hipFuncAttributeMaxDynamicSharedMemorySize, LDS_BYTES);
  fused_kernel<<<256, 512, LDS_BYTES, stream>>>((const char*)d_in[0], (const char*)d_in[1], (const char*)d_in[2],
                                                (const char*)d_in[3], (const char*)d_in[5], (const char*)d_in[6],
                                                (const char*)d_in[7], (const char*)d_in[8], (char*)d_out, (char*)d_ws);
}
```

```cpp
#include <hip/hip_runtime.h>
#include <stdint.h>

#define EXP_NODMA 0
#define EXP_NOBAR 0
typedef unsigned int u32;
typedef unsigned long long u64;
typedef __bf16 bf16x8 __attribute__((ext_vector_type(8)));
typedef __bf16 bf16x2 __attribute__((ext_vector_type(2)));
typedef short v4i16 __attribute__((ext_vector_type(4)));
typedef float f32x4 __attribute__((ext_vector_type(4)));
typedef float f32x2 __attribute__((ext_vector_type(2)));
typedef u32 u32x4 __attribute__((ext_vector_type(4)));
typedef u32 u32x2 __attribute__((ext_vector_type(2)));

#define AS1 __attribute__((address_space(1)))
#define AS3 __attribute__((address_space(3)))
#define AS4 __attribute__((address_space(4)))
#define DEVI __device__ __forceinline__

constexpr u32 WS_EPOCH = 0;
constexpr u32 WS_AFLAG = 1024;
constexpr u32 WS_HFLAG = 2048;
constexpr u32 WS_OFLAG = 4096;
constexpr u32 WS_STAT = 8192;
constexpr u32 WS_CS = 40960;
constexpr u32 WS_KVFLAG = 0x4A000;
constexpr u32 WS_KVP = 0x50000;
constexpr u32 WS_ZP = 0xD0000;
constexpr u32 WS_AF = 0x100000;
constexpr u32 WS_TIM = 0xB00000;
constexpr u32 WS_USED = 0xC00000;

constexpr u32 SLOT = 49152;
constexpr u32 L_SCR = 131072;
constexpr u32 L_ZR = L_SCR + 24576;
constexpr u32 L_CSB = 157696;
constexpr u32 L_MEAN = 159744;
constexpr u32 L_RSTD = 160256;
constexpr u32 L_WPHI = 160768;
constexpr u32 L_PF = 162816;
constexpr u32 L_OFL = 163072;
constexpr u32 LDS_BYTES = 163328;
constexpr u32 CODE_LINES = 296;

DEVI f32x4 mfma32(u32x4 a, u32x4 b, f32x4 c) {
  return __builtin_amdgcn_mfma_f32_16x16x32_bf16(__builtin_bit_cast(bf16x8, a), __builtin_bit_cast(bf16x8, b), c, 0, 0, 0);
}
DEVI f32x4 mfma16(u32x2 a, u32x2 b, f32x4 c) {
  return __builtin_amdgcn_mfma_f32_16x16x16bf16_1k(__builtin_bit_cast(v4i16, a), __builtin_bit_cast(v4i16, b), c, 0, 0, 0);
}
DEVI u32 pk2(float a, float b) {
  f32x2 v = {a, b};
  return __builtin_bit_cast(u32, __builtin_convertvector(v, bf16x2));
}
DEVI float bflo(u32 v) { return __builtin_bit_cast(float, v << 16); }
DEVI float bfhi(u32 v) { return __builtin_bit_cast(float, v & 0xffff0000u); }
DEVI u32 fbits(float f) { return __builtin_bit_cast(u32, f); }
DEVI float bitsf(u32 u) { return __builtin_bit_cast(float, u); }

DEVI u32x2 lds_tr(AS3 char* L, u32 addr) {
  v4i16 t = __builtin_amdgcn_ds_read_tr16_b64_v4i16((AS3 v4i16*)(L + addr));
  return __builtin_bit_cast(u32x2, t);
}
DEVI u32x4 lds_frag_tr(AS3 char* L, u32 addr, u32 second) {
  u32x2 a = lds_tr(L, addr), b = lds_tr(L, addr + second);
  u32x4 r = {a[0], a[1], b[0], b[1]};
  return r;
}
DEVI u32x4 lds128(AS3 char* L, u32 addr) { return *(AS3 u32x4*)(L + addr); }
DEVI f32x4 lds128f(AS3 char* L, u32 addr) { return *(AS3 f32x4*)(L + addr); }
template <int OFF> DEVI void glds_m0(u32 m0v, const char* sbase, u32 voff) {
  asm volatile("s_mov_b32 m0, %0\n\ts_nop 0\n\tglobal_load_lds_dwordx4 %1, %2 offset:%3" ::"s"(m0v), "v"(voff), "s"(sbase), "n"(OFF) : "memory");
}
template <int OFF> DEVI void glds(const char* sbase, u32 voff) {
  asm volatile("global_load_lds_dwordx4 %0, %1 offset:%2" ::"v"(voff), "s"(sbase), "n"(OFF) : "memory");
}
template <int OFF> DEVI void glds_masked(const char* sbase, u32 voff, u64 mask) {
  asm volatile("s_mov_b64 exec, %3\n\tglobal_load_lds_dwordx4 %0, %1 offset:%2\n\ts_mov_b64 exec, -1" ::"v"(voff), "s"(sbase), "n"(OFF), "s"(mask) : "memory");
}
DEVI void pf_lds(u32 m0v, const char* sbase, u32 voff, u64 mask) {
  asm volatile("s_mov_b32 m0, %0\n\ts_mov_b64 exec, %3\n\tglobal_load_lds_dword %1, %2\n\ts_mov_b64 exec, -1" ::"s"(m0v), "v"(voff), "s"(sbase), "s"(mask) : "memory");
}
template <int OFF> DEVI void dsr128(u32x4& d, u32 a) {
  asm volatile("ds_read_b128 %0, %1 offset:%2" : "=v"(d) : "v"(a), "n"(OFF) : "memory");
}
template <int OFF> DEVI void dsrtr(u32x2& d, u32 a) {
  asm volatile("ds_read_b64_tr_b16 %0, %1 offset:%2" : "=v"(d) : "v"(a), "n"(OFF) : "memory");
}
DEVI u32x4 cat(u32x2 a, u32x2 b) { u32x4 r = {a[0], a[1], b[0], b[1]}; return r; }
#define WAIT_V(n) asm volatile("s_waitcnt vmcnt(%0)" ::"n"(n) : "memory")
#define WAIT_L0() asm volatile("s_waitcnt lgkmcnt(0)" ::: "memory")
#define BAR() __builtin_amdgcn_s_barrier()
#define SCHED() __builtin_amdgcn_sched_barrier(0)

DEVI void ld_sc1_x4(u32x4& d, const char* base, u32 off) {
  asm volatile("global_load_dwordx4 %0, %1, %2 sc1" : "=v"(d) : "v"(off), "s"(base) : "memory");
}
DEVI void ld_x4(u32x4& d, const char* base, u32 off) {
  asm volatile("global_load_dwordx4 %0, %1, %2" : "=v"(d) : "v"(off), "s"(base) : "memory");
}
DEVI void ld_x1(u32& d, const char* base, u32 off) {
  asm volatile("global_load_dword %0, %1, %2" : "=v"(d) : "v"(off), "s"(base) : "memory");
}
DEVI void ld_u16(u32& d, const char* base, u32 off) {
  asm volatile("global_load_ushort %0, %1, %2" : "=v"(d) : "v"(off), "s"(base) : "memory");
}
DEVI u32 ld_sc1_x1_wait(const char* base, u32 off) {
  u32 d;
  asm volatile("global_load_dword %0, %1, %2 sc1\n\ts_waitcnt vmcnt(0)" : "=v"(d) : "v"(off), "s"(base) : "memory");
  return d;
}
DEVI void st_sc1_x4(char* base, u32 off, u32x4 v) {
  asm volatile("global_store_dwordx4 %0, %1, %2 sc1\n\ts_nop 1" ::"v"(off), "v"(v), "s"(base) : "memory");
}
DEVI void st_sc1_x2(char* base, u32 off, u32x2 v) {
  asm volatile("global_store_dwordx2 %0, %1, %2 sc1" ::"v"(off), "v"(v), "s"(base) : "memory");
}
DEVI void st_sc1_x1(char* base, u32 off, u32 v) {
  asm volatile("global_store_dword %0, %1, %2 sc1" ::"v"(off), "v"(v), "s"(base) : "memory");
}
constexpr int SPIN_MAX = 1 << 17;
DEVI void poll_flags(const char* base, u32 off0, int n, int l, u32 tag) {
  u32 off = off0 + (u32)(l < n ? l : 0) * 4u;
#pragma unroll 1
  for (int it = 0; it < SPIN_MAX; ++it) {
    u32 v = ld_sc1_x1_wait(base, off);
    if (__builtin_amdgcn_ballot_w64(v != tag) == 0ull) break;
    __builtin_amdgcn_s_sleep(1);
  }
  __builtin_amdgcn_fence(__ATOMIC_ACQUIRE, "wavefront");
}

#define POLL_SLEEP 1
DEVI void poll_o(const char* base, u32 off, u32 tag) {
#pragma unroll 1
  for (int it = 0; it < SPIN_MAX; ++it) {
    u32 v = ld_sc1_x1_wait(base, off);
    if (__builtin_amdgcn_ballot_w64(v != tag) == 0ull) break;
    __builtin_amdgcn_s_sleep(POLL_SLEEP);
  }
  __builtin_amdgcn_fence(__ATOMIC_ACQUIRE, "wavefront");
}

#define STAMP(i) do {} while (0)
#define STAMPG(i) do {} while (0)
#define STAMP_DECL do {} while (0)
#define STAMP_FLUSH do {} while (0)

DEVI void red4_max2(float& a, float& b) {
  auto s = __builtin_amdgcn_permlane32_swap(fbits(a), fbits(b), false, false);
  float t = fmaxf(bitsf(s[0]), bitsf(s[1]));
  auto u = __builtin_amdgcn_permlane16_swap(fbits(t), fbits(t), false, false);
  float m = fmaxf(bitsf(u[0]), bitsf(u[1]));
  auto z = __builtin_amdgcn_permlane32_swap(fbits(m), fbits(m), false, false);
  a = bitsf(z[0]); b = bitsf(z[1]);
}
DEVI void red4_sum2_rcp(float& a, float& b) {
  auto s = __builtin_amdgcn_permlane32_swap(fbits(a), fbits(b), false, false);
  float t = bitsf(s[0]) + bitsf(s[1]);
  auto u = __builtin_amdgcn_permlane16_swap(fbits(t), fbits(t), false, false);
  float m = __builtin_amdgcn_rcpf(bitsf(u[0]) + bitsf(u[1]));
  auto z = __builtin_amdgcn_permlane32_swap(fbits(m), fbits(m), false, false);
  a = bitsf(z[0]); b = bitsf(z[1]);
}
DEVI u32x4 dual_softmax(f32x4 p) {
  const float L2E = 1.4426950408889634f;
  float mx = fmaxf(fmaxf(p[0], p[1]), fmaxf(p[2], p[3]));
  float nm = fmaxf(fmaxf(-p[0], -p[1]), fmaxf(-p[2], -p[3]));
  red4_max2(mx, nm);
  float a = -mx * L2E, c = -nm * L2E;
  float e0 = __builtin_amdgcn_exp2f(fmaf(p[0], L2E, a)), e1 = __builtin_amdgcn_exp2f(fmaf(p[1], L2E, a));
  float e2 = __builtin_amdgcn_exp2f(fmaf(p[2], L2E, a)), e3 = __builtin_amdgcn_exp2f(fmaf(p[3], L2E, a));
  float n0 = __builtin_amdgcn_exp2f(fmaf(p[0], -L2E, c)), n1 = __builtin_amdgcn_exp2f(fmaf(p[1], -L2E, c));
  float n2 = __builtin_amdgcn_exp2f(fmaf(p[2], -L2E, c)), n3 = __builtin_amdgcn_exp2f(fmaf(p[3], -L2E, c));
  float sp = (e0 + e1) + (e2 + e3), sn = (n0 + n1) + (n2 + n3);
  red4_sum2_rcp(sp, sn);
  u32x4 r = {pk2(e0 * sp, e1 * sp), pk2(e2 * sp, e3 * sp), pk2(n0 * sn, n1 * sn), pk2(n2 * sn, n3 * sn)};
  return r;
}

#define PF_MODE 2
constexpr int PFN = PF_MODE == 1 ? 2 : 0;
template <int V> struct IC { static constexpr int value = V; };
template <int N> DEVI void wait4(u32x4& a, u32x4& b, u32x4& c, u32x4& d) {
  asm volatile("s_waitcnt vmcnt(%4)" : "+v"(a), "+v"(b), "+v"(c), "+v"(d) : "n"(N) : "memory");
}
template <int N> DEVI void wait1(u32x4& a) { asm volatile("s_waitcnt vmcnt(%1)" : "+v"(a) : "n"(N) : "memory"); }

template <int N> DEVI void waitset(u32x4& a, u32x4& b, u32x2 (&lo)[8], u32x2 (&hi)[8]);
template <> DEVI void waitset<8>(u32x4& a, u32x4& b, u32x2 (&lo)[8], u32x2 (&hi)[8]) {
  asm volatile("s_waitcnt lgkmcnt(0)"
               : "+v"(a), "+v"(b), "+v"(lo[0]), "+v"(lo[1]), "+v"(lo[2]), "+v"(lo[3]), "+v"(lo[4]), "+v"(lo[5]), "+v"(lo[6]), "+v"(lo[7]),
                 "+v"(hi[0]), "+v"(hi[1]), "+v"(hi[2]), "+v"(hi[3]), "+v"(hi[4]), "+v"(hi[5]), "+v"(hi[6]), "+v"(hi[7])
               :: "memory");
}
template <> DEVI void waitset<6>(u32x4& a, u32x4& b, u32x2 (&lo)[8], u32x2 (&hi)[8]) {
  asm volatile("s_waitcnt lgkmcnt(0)"
               : "+v"(a), "+v"(b), "+v"(lo[0]), "+v"(lo[1]), "+v"(lo[2]), "+v"(lo[3]), "+v"(lo[4]), "+v"(lo[5]),
                 "+v"(hi[0]), "+v"(hi[1]), "+v"(hi[2]), "+v"(hi[3]), "+v"(hi[4]), "+v"(hi[5])
               :: "memory");
}
DEVI void waitb(u32x2 (&lo)[4], u32x2 (&hi)[4]) {
  asm volatile("s_waitcnt lgkmcnt(0)"
               : "+v"(lo[0]), "+v"(lo[1]), "+v"(lo[2]), "+v"(lo[3]), "+v"(hi[0]), "+v"(hi[1]), "+v"(hi[2]), "+v"(hi[3]) :: "memory");
}
DEVI float wave_sum(float v) {
#define DPP_ADD(ctrl, rmask) v += bitsf((u32)__builtin_amdgcn_update_dpp(0, (int)fbits(v), ctrl, rmask, 0xf, false))
  DPP_ADD(0x111, 0xf); DPP_ADD(0x112, 0xf); DPP_ADD(0x114, 0xf); DPP_ADD(0x118, 0xf);
  DPP_ADD(0x142, 0xa); DPP_ADD(0x143, 0xc);
#undef DPP_ADD
  return bitsf((u32)__builtin_amdgcn_readlane((int)fbits(v), 63));
}

extern "C" __global__ void __launch_bounds__(512, 2)
fused_kernel(const char* __restrict__ x, const char* __restrict__ wqkv, const char* __restrict__ wphi,
             const char* __restrict__ wo, const char* __restrict__ w1, const char* __restrict__ b1,
             const char* __restrict__ w2, const char* __restrict__ b2, char* __restrict__ out, char* __restrict__ ws) {
  extern __shared__ __attribute__((aligned(16))) char smem[];
  AS3 char* L = (AS3 char*)smem;
  const int b = blockIdx.x;
  const int l = threadIdx.x & 63;
  const int w = __builtin_amdgcn_readfirstlane(threadIdx.x >> 6);
  u32 pfd;
  {
    u64 pc;
    asm volatile("s_getpc_b64 %0" : "=s"(pc));
    const u32 ln = (u32)(w * 40 + (l < 40 ? l : 39));
    ld_x1(pfd, (const char*)(pc & ~127ull), (ln < CODE_LINES ? ln : CODE_LINES - 1u) * 128u);
  }
  asm volatile("" ::"s"(x), "s"(wqkv), "s"(wphi), "s"(wo), "s"(w1), "s"(b1), "s"(w2), "s"(b2), "s"(out), "s"(ws));
  const int xcd = b & 7, bi = b >> 3;
  const int p = (xcd >> 2) * 8 + (bi >> 2), c = (xcd & 3) * 4 + (bi & 3), g = c >> 1;
  const bool typeA = (c & 1) == 0;
  const int rg = w & 3, cg = w >> 2;
  const int gq = l >> 4, r16 = l & 15, q4 = (l >> 2) & 3, pp = l & 3;
  const int rot = (p & 1) * 4;
  const int rb = 4 * xcd + (bi >> 3), cb = bi & 7;
  STAMP_DECL;
  STAMP(0);

  const u32 lb = (u32)(size_t)L;
  const u32 xrow_off = (u32)(128 * p + 8 * c + w) * 1024u + (u32)l * 16u;
  u32 gb1[2];
  const u32 gb1_off = (u32)(224 * g + (w < 6 ? 32 * w + ((g & 1) ? 32 : 0) : (w == 6 && !(g & 1) ? 192 : 0)) + (l >> 2)) * 2u;
  u32 tag;

  const char* xsrc = x + (size_t)p * 131072u;
  const u32 wstride = typeA ? 3072u : 3584u;
  const char* wsrc = (typeA ? (wqkv + g * 128) : (w1 + 448 * g)) - 4096;
  const u32 vx0 = (u32)(16 * w + (l >> 3)) * 1024u + (u32)(((l & 7) ^ (l >> 4)) * 16);
  const u32 vx1 = (u32)(16 * w + 7 + (l >> 3)) * 1024u + (u32)(((l & 7) ^ (4 + (l >> 4))) * 16);
  const u32 wsp = (u32)(((l & 7) >> 1) ^ (((l >> 4) & 1) | ((w & 1) << 1)));
  const u32 vw = (u32)(8 * w + (l >> 3)) * wstride + (wsp * 2u + (u32)(l & 1)) * 16u + 4096u;
  u32 vws[4];
  {
    const u32 odd = (u32)(g & 1) * 64u;
    vws[0] = typeA ? vw : vw + odd;
    vws[1] = typeA ? vw : vw + odd + 128u - 1024u;
    vws[2] = typeA ? vw : vw + odd + 256u - 2048u;
    vws[3] = vw + (odd ? 0u : 384u) - 3072u;
  }
  const u64 hmask = __builtin_amdgcn_ballot_w64(wsp < 2u);
  const u32 m0x = lb + (u32)w * 2048u, m0w = lb + 16384u + (u32)w * 4096u;

  const char* dsrc2 = w2 + (size_t)(7 * w) * 32768u + cb * 128;
  const char* dsrco = wo + (size_t)(2 * w) * 32768u + cb * 128;
  const u32 vdb = (u32)(l >> 3) * 1024u + (u32)(((((l & 7) >> 1) ^ ((l >> 4) & 3)) * 2 + (l & 1)) * 16);
  auto dslot = [&](int j) -> u32 { return (u32)((j + 3) & 3) * 32768u + (u32)w * 4096u; };
  auto issueB = [&](int j) {
    const char* s = (j < 7) ? (dsrc2 + j * 32768) : (dsrco + (j - 7) * 32768);
    glds_m0<0>((u32)__builtin_amdgcn_readfirstlane((int)(lb + dslot(j))), s, vdb);
    glds<1024>(s, vdb + 7168u);
    glds<2048>(s, vdb + 2u * 7168u);
    glds<3072>(s, vdb + 3u * 7168u);
  };

  auto issueBk = [&](int j, int k) {
    const char* s = (j < 7) ? (dsrc2 + j * 32768) : (dsrco + (j - 7) * 32768);
    if (k == 0) glds_m0<0>(lb + dslot(j), s, vdb);
    if (k == 1) glds<1024>(s, vdb + 7168u);
    if (k == 2) glds<2048>(s, vdb + 2u * 7168u);
    if (k == 3) glds<3072>(s, vdb + 3u * 7168u);
  };

  auto prefetchW = [&]() {
    const u32 wi = (u32)((bi >> 1) * 8 + w);
    const u32 o2 = (wi * 14u) * 1024u + (u32)(l < 56 ? l : 0) * 128u;
    pf_lds(lb + L_PF, w2, o2, 0x00ffffffffffffffull);
    pf_lds(lb + L_PF, w2, o2 + 7168u, 0x00ffffffffffffffull);
    pf_lds(lb + L_PF, wo, (wi * 4u) * 1024u + (u32)(l & 31) * 128u, 0xffffffffull);
  };
  u32 xa0 = lb + (u32)(32 * rg + r16) * 128u + (u32)((gq ^ ((r16 >> 1) & 7)) * 16);
  u32 xa1 = xa0 ^ 64u;
  const u32 vbase = lb + 16384u + (u32)gq * 4096u + (u32)q4 * 128u + (u32)pp * 8u + (u32)(((q4 >> 1) & 1) | ((gq & 1) << 1)) * 32u;
  u32 R[8];
#pragma unroll
  for (int t = 0; t < 8; ++t) {
    const u32 ra = (vbase ^ (u32)((2 * cg + (t & 1)) * 32)) + (u32)(t >> 1) * 1024u;
    const u32 rbb = (vbase ^ (u32)((t & 3) * 32)) + (u32)(t >> 2) * 1024u + (u32)cg * 2048u;
    R[t] = typeA ? ra : rbb;
  }

  f32x4 acc[8][2];
#pragma unroll
  for (int t = 0; t < 8; ++t) { acc[t][0] = f32x4{0, 0, 0, 0}; acc[t][1] = f32x4{0, 0, 0, 0}; }
  u32x4 gat[8];
  const u32x4 ones = {0x3F803F80u, 0x3F803F80u, 0x3F803F80u, 0x3F803F80u};
  const u32 gcs = WS_CS + (u32)(c * 256 + 32 * w + (l >> 2)) * 64u + (u32)(l & 3) * 16u;
  const u32 gst = WS_STAT + (u32)(128 * p + 16 * w + r16) * 16u;

  auto g1 = [&](auto TAc, auto N8c) {
    constexpr bool TA = decltype(TAc)::value != 0, N8 = decltype(N8c)::value != 0;
    constexpr int NG = TA ? 5 : 6;
    u32x4 fx[2][2];
    u32x2 flo[2][8], fhi[2][8];
    u32x4 xrow;
    u32 epoch;
    asm volatile("s_load_dword %0, %1, 0x0" : "=s"(epoch) : "s"(ws) : "memory");
    ld_x4(xrow, x, xrow_off);
    if (TA) { gb1[0] = 0; gb1[1] = 0; }
    else { ld_u16(gb1[0], b1, gb1_off); ld_u16(gb1[1], b1, gb1_off + 32u); }
    auto issue = [&](int s, u32 sb) {
      const int ks = (s + rot) & 7;
      const char* xs = xsrc + ks * 128;
      const char* wsb = wsrc + (size_t)((u32)ks * 64u * wstride);
      glds_m0<0>(m0x + sb, xs, vx0);
      glds<1024>(xs, vx1);
      glds_m0<0>(m0w + sb, wsb, vws[0]);
      glds<1024>(wsb, vws[1]);
      glds<2048>(wsb, vws[2]);
      if (!TA) glds_masked<3072>(wsb, vws[3], hmask);
    };
    issue(0, 0); issue(1, SLOT); issue(2, 2 * SLOT);
    if (TA && w < 2) glds_m0<0>(lb + L_WPHI + (u32)w * 1024u, wphi + g * 2048 + w * 1024, (u32)l * 16u);

#define MF(S, t, rt) acc[t][rt] = mfma32(cat(flo[S][t], fhi[S][t]), fx[S][rt], acc[t][rt])
#define MFS(S, t, rt) acc[t][rt] = mfma32(fx[S][rt], cat(flo[S][t], fhi[S][t]), acc[t][rt])
#define RW(N, t) do { if (RD) { dsrtr<(N) * 16384>(flo[N][t], R[t]); dsrtr<(N) * 16384 + 512>(fhi[N][t], R[t]); } } while (0)
#define RX(N) do { if (RD) { dsr128<0>(fx[N][0], (N) ? xa1 : xa0); dsr128<2048>(fx[N][1], (N) ? xa1 : xa0); } } while (0)
    auto waitS = [&](auto Sc) {
      constexpr int S = decltype(Sc)::value;
      if constexpr (N8) waitset<8>(fx[S][0], fx[S][1], flo[S], fhi[S]);
      else waitset<6>(fx[S][0], fx[S][1], flo[S], fhi[S]);
    };
    auto phase = [&](auto Sc, auto RDc, auto GLc, const char* xs, const char* wsb, u32 sb) {
      constexpr int S = decltype(Sc)::value, N = 1 - S;
      constexpr bool RD = decltype(RDc)::value != 0;
      constexpr int GL = decltype(GLc)::value;
      waitS(Sc);
      if (S == 1 && RD && !EXP_NOBAR) BAR();
      SCHED();
#define DM(i) do { if (EXP_NODMA) break; \
        if (GL == 1 && (i) == 0) glds_m0<0>(m0x + sb, xs, vx0); \
        if (GL == 1 && (i) == 1) glds<1024>(xs, vx1); \
        if (GL == 1 && (i) == 2) glds_m0<0>(m0w + sb, wsb, vws[0]); \
        if (GL == 2 && (i) == 0) glds_m0<1024>(m0w + sb, wsb, vws[1]); \
        if (GL == 2 && (i) == 1) glds<2048>(wsb, vws[2]); \
        if (GL == 2 && (i) == 2 && !TA) glds_masked<3072>(wsb, vws[3], hmask); } while (0)
      if constexpr (N8) {
        MF(S, 6, 0); RW(N, 6); SCHED();
        MF(S, 6, 1); RW(N, 7); SCHED();
        MF(S, 7, 0); DM(0); SCHED();
        MF(S, 7, 1); SCHED();
      }
      if constexpr (TA) {
        MFS(S, 4, 0); RX(N); SCHED();
        MFS(S, 4, 1); RW(N, 4); SCHED();
        MFS(S, 5, 0); if (!N8) DM(0); SCHED();
        MFS(S, 5, 1); RW(N, 5); SCHED();
      } else {
        MF(S, 4, 0); RX(N); SCHED();
        MF(S, 4, 1); RW(N, 4); SCHED();
        MF(S, 5, 0); if (!N8) DM(0); SCHED();
        MF(S, 5, 1); RW(N, 5); SCHED();
      }
      MF(S, 0, 0); RW(N, 0); SCHED();
      MF(S, 0, 1); RW(N, 1); SCHED();
      MF(S, 1, 0); DM(1); SCHED();
      MF(S, 1, 1); RW(N, 2); SCHED();
      MF(S, 2, 0); RW(N, 3); SCHED();
      MF(S, 2, 1); SCHED();
      MF(S, 3, 0); DM(2); SCHED();
      MF(S, 3, 1); SCHED();
#undef DM
    };
    auto advance = [&](u32 d) {
#pragma unroll
      for (int t = 0; t < (N8 ? 8 : 6); ++t) R[t] += d;
      xa0 += d; xa1 += d;
    };

    STAMP(1);
    if (TA) asm volatile("s_waitcnt vmcnt(%1)" : "+v"(xrow) : "n"(2 * NG) : "memory");
    else asm volatile("s_waitcnt vmcnt(%3)" : "+v"(xrow), "+v"(gb1[0]), "+v"(gb1[1]) : "n"(2 * NG) : "memory");
    asm volatile("" ::"v"(pfd));
    STAMPG(1);
    BAR();
    STAMPG(2);
    {
      constexpr bool RD = true;
      RX(0); RW(0, 4); RW(0, 5); RW(0, 0); RW(0, 1); RW(0, 2); RW(0, 3);
      if constexpr (N8) { RW(0, 6); RW(0, 7); }
    }
    waitS(IC<0>{});
    asm volatile("" : "+s"(epoch) :: "memory");
    tag = epoch + 1u;
    if (threadIdx.x == 0) *(volatile AS3 u32*)(L + L_OFL) = epoch;
    phase(IC<0>{}, IC<1>{}, IC<0>{}, nullptr, nullptr, 0u);
    {
      float v[8];
#pragma unroll
      for (int i = 0; i < 4; ++i) { v[2 * i] = bflo(xrow[i]); v[2 * i + 1] = bfhi(xrow[i]); }
      const float mean = wave_sum(((v[0] + v[1]) + (v[2] + v[3])) + ((v[4] + v[5]) + (v[6] + v[7]))) * (1.0f / 512.0f);
      float qq = 0.f;
#pragma unroll
      for (int i = 0; i < 8; ++i) { float d = v[i] - mean; qq = fmaf(d, d, qq); }
      const float rstd = 1.0f / sqrtf(wave_sum(qq) * (1.0f / 512.0f) + 1e-5f);
      if (l == 0) {
        u32x4 gr = {fbits(mean), fbits(rstd), tag, tag};
        st_sc1_x4(ws, WS_STAT + (u32)(128 * p + 8 * c + w) * 16u, gr);
        if (w == 7) st_sc1_x1(ws, WS_AFLAG + (u32)b * 4u, tag);
      }
    }
    auto colsum = [&](int s, u32 sb) {
      if (p < 8 && (p >> 1) == s && w < (TA ? 6 : 7)) {
        u32x2 c0l[2], c0h[2], c1l[2], c1h[2];
        const u32 a0 = (vbase ^ (u32)((2 * (w & 1)) * 32)) + (u32)(w >> 1) * 1024u + sb;
        const u32 a1 = a0 ^ 32u;
        dsrtr<0>(c0l[0], a0); dsrtr<512>(c0h[0], a0); dsrtr<16384>(c0l[1], a0); dsrtr<16384 + 512>(c0h[1], a0);
        dsrtr<0>(c1l[0], a1); dsrtr<512>(c1h[0], a1); dsrtr<16384>(c1l[1], a1); dsrtr<16384 + 512>(c1h[1], a1);
        asm volatile("s_waitcnt lgkmcnt(0)" : "+v"(c0l[0]), "+v"(c0h[0]), "+v"(c0l[1]), "+v"(c0h[1]),
                     "+v"(c1l[0]), "+v"(c1h[0]), "+v"(c1l[1]), "+v"(c1h[1]) :: "memory");
        f32x4 c0 = {0, 0, 0, 0}, c1 = {0, 0, 0, 0};
#pragma unroll
        for (int ks = 0; ks < 2; ++ks) {
          c0 = mfma32(ones, cat(c0l[ks], c0h[ks]), c0);
          c1 = mfma32(ones, cat(c1l[ks], c1h[ks]), c1);
        }
        if (l < 32) {
          u32x2 gr = {fbits(l < 16 ? c0[0] : c1[0]), tag};
          st_sc1_x2(ws, WS_CS + (u32)((c * 256 + 32 * w + l) * 8 + ((s + rot) & 7)) * 8u, gr);
        }
      }
    };
    colsum(0, 0u);
    u32 sb = 0;
#pragma unroll 1
    for (int s = 0; s < 5; ++s) {
      const u32 nsb = sb == 2 * SLOT ? 0u : sb + SLOT;
      advance(nsb - sb);
      const int ks = (s + 3 + rot) & 7;
      WAIT_V(NG);
      STAMPG(3 + s);
      phase(IC<1>{}, IC<1>{}, IC<1>{}, xsrc + ks * 128, wsrc + (size_t)((u32)ks * 64u * wstride), sb);
      phase(IC<0>{}, IC<1>{}, IC<2>{}, xsrc + ks * 128, wsrc + (size_t)((u32)ks * 64u * wstride), sb);
      colsum(s + 1, nsb);
      sb = nsb;
    }
    advance(0u - 2u * SLOT);
    WAIT_V(NG);
    STAMPG(8);
    ld_sc1_x4(gat[0], ws, gcs);
    ld_sc1_x4(gat[1], ws, gcs + 1024u);
    ld_sc1_x4(gat[2], ws, gst);
    if (PF_MODE == 1) prefetchW();
    phase(IC<1>{}, IC<1>{}, IC<0>{}, nullptr, nullptr, 0u);
    if (TA) issueB(0);
    phase(IC<0>{}, IC<1>{}, IC<0>{}, nullptr, nullptr, 0u);
    advance(SLOT);
    WAIT_V((TA ? 7 : 3) + PFN);
    STAMPG(9);
    phase(IC<1>{}, IC<1>{}, IC<0>{}, nullptr, nullptr, 0u);
    if (TA) issueB(1);
    phase(IC<0>{}, IC<1>{}, IC<0>{}, nullptr, nullptr, 0u);
    asm volatile("s_waitcnt vmcnt(%3)" : "+v"(gat[0]), "+v"(gat[1]), "+v"(gat[2]) : "n"((TA ? 8 : 0) + PFN) : "memory");
    {
      const bool used = w < (TA ? 6 : 7);
#pragma unroll 1
      for (int it = 0; it < SPIN_MAX; ++it) {
        const bool bad = (used && ((gat[0][1] != tag) | (gat[0][3] != tag) | (gat[1][1] != tag) | (gat[1][3] != tag))) | (gat[2][2] != tag);
        if (__builtin_amdgcn_ballot_w64(bad) == 0ull) break;
        ld_sc1_x4(gat[0], ws, gcs);
        ld_sc1_x4(gat[1], ws, gcs + 1024u);
        ld_sc1_x4(gat[2], ws, gst);
        asm volatile("s_waitcnt vmcnt(0)" : "+v"(gat[0]), "+v"(gat[1]), "+v"(gat[2]) :: "memory");
      }
#pragma unroll
      for (int i = 0; i < 2; ++i) {
        float sm = bitsf(gat[i][0]) + bitsf(gat[i][2]);
        sm += bitsf((u32)__builtin_amdgcn_update_dpp(0, (int)fbits(sm), 0xB1, 0xf, 0xf, false));
        sm += bitsf((u32)__builtin_amdgcn_update_dpp(0, (int)fbits(sm), 0x4E, 0xf, 0xf, false));
        if ((l & 3) == 0) {
          f32x2 o = {sm, bflo(gb1[i])};
          *(AS3 f32x2*)(L + L_CSB + (u32)(32 * w + 16 * i + (l >> 2)) * 8u) = o;
        }
      }
      if (l < 16) {
        *(AS3 float*)(L + L_MEAN + (u32)(16 * w + l) * 4u) = bitsf(gat[2][0]);
        *(AS3 float*)(L + L_RSTD + (u32)(16 * w + l) * 4u) = bitsf(gat[2][1]);
      }
    }
    waitS(IC<1>{});
    BAR();
    SCHED();
    if constexpr (N8) { MF(1, 6, 0); MF(1, 6, 1); MF(1, 7, 0); MF(1, 7, 1); SCHED(); }
    if constexpr (TA) {
      MFS(1, 4, 0); issueBk(2, 0); SCHED(); MFS(1, 4, 1); issueBk(2, 1); SCHED();
      MFS(1, 5, 0); issueBk(2, 2); SCHED(); MFS(1, 5, 1); issueBk(2, 3); SCHED();
    } else {
      MF(1, 4, 0); MF(1, 4, 1); MF(1, 5, 0); MF(1, 5, 1); SCHED();
    }
    MF(1, 2, 0); if (TA) issueBk(3, 0); SCHED(); MF(1, 2, 1); if (TA) issueBk(3, 1); SCHED();
    MF(1, 3, 0); if (TA) issueBk(3, 2); SCHED(); MF(1, 3, 1); if (TA) issueBk(3, 3); SCHED();
    MF(1, 0, 0); MF(1, 0, 1); MF(1, 1, 0); MF(1, 1, 1); SCHED();
#undef MF
#undef MFS
#undef RW
#undef RX
    STAMP(2);
    STAMPG(10);
  };
  if (typeA) g1(IC<1>{}, IC<0>{});
  else if (cg == 0) g1(IC<0>{}, IC<1>{});
  else g1(IC<0>{}, IC<0>{});
  STAMP(3);

  const float mean0 = *(AS3 float*)(L + L_MEAN + (u32)(32 * rg + r16) * 4u);
  const float mean1 = *(AS3 float*)(L + L_MEAN + (u32)(32 * rg + 16 + r16) * 4u);
  const float rstd0 = *(AS3 float*)(L + L_RSTD + (u32)(32 * rg + r16) * 4u);
  const float rstd1 = *(AS3 float*)(L + L_RSTD + (u32)(32 * rg + 16 + r16) * 4u);

  const u32 vb2 = (u32)(4 * gq + q4) * 128u + (u32)pp * 8u + (u32)((2 * gq + (q4 >> 1)) & 3) * 32u;
  u32x2 blo[2][4], bhi[2][4];
  u32 VB[4];
#pragma unroll
  for (int ct = 0; ct < 4; ++ct) VB[ct] = lb + (vb2 ^ (u32)(ct * 32)) + (u32)w * 4096u;
  auto rdB = [&](auto jc) {
    constexpr int j = decltype(jc)::value, blk = (j + 3) & 3;
#pragma unroll
    for (int ct = 0; ct < 4; ++ct) {
      const u32 a = VB[ct] + (blk >= 2 ? 65536u : 0u);
      dsrtr<(blk & 1) * 32768>(blo[j & 1][ct], a);
      dsrtr<(blk & 1) * 32768 + 2048>(bhi[j & 1][ct], a);
    }
  };
  const int dp = rb >> 1;

  const char* af = ws + WS_AF + (size_t)(4 * rb) * 73728u;
  const char* afh = af + (size_t)(7 * w) * 1024u;
  const char* afo = af + (size_t)(56 + 2 * w) * 1024u;
  const u32 vo = (u32)l * 16u;
  u32x4 a[9][4];
  auto loadA = [&](int j) {
    const char* s = j < 7 ? afh + j * 1024 : afo + (j - 7) * 1024;
#pragma unroll
    for (int rt = 0; rt < 4; ++rt) ld_sc1_x4(a[j][rt], s + rt * 73728, vo);
  };
  auto waitA05 = [&]() {
    asm volatile("s_waitcnt vmcnt(0)" : "+v"(a[0][0]), "+v"(a[0][1]), "+v"(a[0][2]), "+v"(a[0][3]), "+v"(a[1][0]), "+v"(a[1][1]), "+v"(a[1][2]), "+v"(a[1][3]),
                 "+v"(a[2][0]), "+v"(a[2][1]), "+v"(a[2][2]), "+v"(a[2][3]), "+v"(a[3][0]), "+v"(a[3][1]), "+v"(a[3][2]), "+v"(a[3][3]) :: "memory");
    asm volatile("" : "+v"(a[4][0]), "+v"(a[4][1]), "+v"(a[4][2]), "+v"(a[4][3]), "+v"(a[5][0]), "+v"(a[5][1]), "+v"(a[5][2]), "+v"(a[5][3]) :: "memory");
  };
  u32x4 phq[2][2], zg;
  if (!typeA) {
    auto hpair = [&](auto Pc) {
      constexpr int P = decltype(Pc)::value;
      const int hl = cg * 4 + P;
      u32x4 o0, o1;
#pragma unroll
      for (int e = 0; e < 2; ++e) {
        const int t = 2 * P + e;
        const u32 ca = L_CSB + (u32)(hl * 32 + e * 16 + 4 * gq) * 8u;
        const f32x4 cb0 = lds128f(L, ca), cb1 = lds128f(L, ca + 16u);
        const float cs[4] = {cb0[0], cb0[2], cb1[0], cb1[2]}, bb[4] = {cb0[1], cb0[3], cb1[1], cb1[3]};
        float h0[4], h1[4];
#pragma unroll
        for (int r = 0; r < 4; ++r) {
          float y0 = fmaf(rstd0, fmaf(-mean0, cs[r], acc[t][0][r]), bb[r]);
          float y1 = fmaf(rstd1, fmaf(-mean1, cs[r], acc[t][1][r]), bb[r]);
          y0 = fmaxf(y0, 0.f); y1 = fmaxf(y1, 0.f);
          h0[r] = y0 * y0; h1[r] = y1 * y1;
        }
        o0[2 * e] = pk2(h0[0], h0[1]); o0[2 * e + 1] = pk2(h0[2], h0[3]);
        o1[2 * e] = pk2(h1[0], h1[1]); o1[2 * e + 1] = pk2(h1[2], h1[3]);
      }
      const u32 step = (u32)(7 * g + ((g & 1) ? (hl < 6 ? hl + 1 : 0) : hl));
      const u32 off = WS_AF + ((u32)(8 * p + 2 * rg) * 72u + step) * 1024u + (u32)l * 16u;
      st_sc1_x4(ws, off, o0);
      st_sc1_x4(ws, off + 72u * 1024u, o1);
    };
    hpair(IC<0>{}); hpair(IC<1>{}); hpair(IC<2>{});
    if (cg == 0) hpair(IC<3>{});
    WAIT_V(0);
    BAR();
    if (threadIdx.x == 0) st_sc1_x1(ws, WS_HFLAG + (u32)p * 128u + (u32)g * 4u, tag);
    STAMP(4);
    if (PF_MODE == 2) prefetchW();
    issueB(0); issueB(1); issueB(2); issueB(3);
    if (w == 0) poll_flags(ws, WS_HFLAG + (u32)dp * 128u, 8, l, tag);
    WAIT_V(0);
    BAR();
    rdB(IC<0>{}); rdB(IC<1>{});
    waitb(blo[0], bhi[0]); waitb(blo[1], bhi[1]);
    issueB(4); issueB(5);
    STAMP(5);
    loadA(0); loadA(1); loadA(2); loadA(3); loadA(4); loadA(5);
    waitA05();
    STAMP(7);
  } else {
    const u32x2 ident = {(4 * gq + 0 == r16 ? 0x3F80u : 0u) | (4 * gq + 1 == r16 ? 0x3F800000u : 0u),
                         (4 * gq + 2 == r16 ? 0x3F80u : 0u) | (4 * gq + 3 == r16 ? 0x3F800000u : 0u)};
    const f32x4 zero4 = {0, 0, 0, 0};
    const f32x4 vm0 = lds128f(L, L_MEAN + (u32)(32 * rg + 4 * gq) * 4u), vm1 = lds128f(L, L_MEAN + (u32)(32 * rg + 16 + 4 * gq) * 4u);
    const f32x4 vr0 = lds128f(L, L_RSTD + (u32)(32 * rg + 4 * gq) * 4u), vr1 = lds128f(L, L_RSTD + (u32)(32 * rg + 16 + 4 * gq) * 4u);
    u32x2 wp[2];
    auto logits = [&](int line, int pi, int rt) -> f32x4 {
      const int t = line * 2 + pi;
      const u32 ca = L_CSB + (u32)(line * 64 + (2 * cg + pi) * 16 + 4 * gq) * 8u;
      const f32x4 cb0 = lds128f(L, ca), cb1 = lds128f(L, ca + 16u);
      const float mn = rt ? mean1 : mean0, rs = rt ? rstd1 : rstd0;
      const float y0 = rs * fmaf(-mn, cb0[0], acc[t][rt][0]), y1 = rs * fmaf(-mn, cb0[2], acc[t][rt][1]);
      const float y2 = rs * fmaf(-mn, cb1[0], acc[t][rt][2]), y3 = rs * fmaf(-mn, cb1[2], acc[t][rt][3]);
      const u32x2 kb = {pk2(y0, y1), pk2(y2, y3)};
      return mfma16(wp[pi], kb, zero4);
    };
#pragma unroll
    for (int pi = 0; pi < 2; ++pi)
      wp[pi] = lds_tr(L, L_WPHI + (u32)((2 * cg + pi) * 512 + (4 * gq + q4) * 32 + pp * 8));
    f32x4 kvt[4], zt[4];
#pragma unroll
    for (int pi = 0; pi < 2; ++pi) {
      u32x4 tp, tn, vv;
      if (pi == 1) { WAIT_V(8); rdB(IC<0>{}); rdB(IC<1>{}); }
#pragma unroll
      for (int rt = 0; rt < 2; ++rt) {
        const u32x4 ph = dual_softmax(logits(1, pi, rt));
        if (pi == 1 && rt == 0) { waitb(blo[0], bhi[0]); waitb(blo[1], bhi[1]); issueB(4); }
        if (pi == 1 && rt == 1) issueB(5);
        const f32x4 a = mfma16(u32x2{ph[0], ph[1]}, ident, zero4);
        const f32x4 n = mfma16(u32x2{ph[2], ph[3]}, ident, zero4);
        tp[2 * rt] = pk2(a[0], a[1]); tp[2 * rt + 1] = pk2(a[2], a[3]);
        tn[2 * rt] = pk2(n[0], n[1]); tn[2 * rt + 1] = pk2(n[2], n[3]);
        const float cs = *(AS3 float*)(L + L_CSB + (u32)(128 + (2 * cg + pi) * 16 + r16) * 8u);
        const f32x4 vm = rt ? vm1 : vm0, vr = rt ? vr1 : vr0;
        const f32x4 av = acc[4 + pi][rt];
        vv[2 * rt] = pk2(vr[0] * fmaf(-vm[0], cs, av[0]), vr[1] * fmaf(-vm[1], cs, av[1]));
        vv[2 * rt + 1] = pk2(vr[2] * fmaf(-vm[2], cs, av[2]), vr[3] * fmaf(-vm[3], cs, av[3]));
      }
      kvt[2 * pi] = mfma32(tp, vv, zero4); kvt[2 * pi + 1] = mfma32(tn, vv, zero4);
      zt[2 * pi] = mfma32(tp, ones, zero4); zt[2 * pi + 1] = mfma32(tn, ones, zero4);
    }
    {
      const int sl = rg > 0 ? rg - 1 : 0;
#pragma unroll
      for (int pi = 0; pi < 2; ++pi) {
        if (rg != pi) {
#pragma unroll
          for (int sg = 0; sg < 2; ++sg)
            *(AS3 f32x4*)(L + L_SCR + (u32)((((cg * 2 + pi) * 2 + sg) * 3 + sl) * 1024 + l * 16)) = kvt[2 * pi + sg];
        }
        if (r16 == 0) {
#pragma unroll
          for (int sg = 0; sg < 2; ++sg)
            *(AS3 f32x4*)(L + L_ZR + (u32)(((((cg * 2 + pi) * 2 + sg) * 4 + rg) * 16 + 4 * gq) * 4)) = zt[2 * pi + sg];
        }
      }
    }
    u32 hv;
    asm volatile("global_load_dword %0, %1, %2 sc1" : "=v"(hv) : "v"(WS_HFLAG + (u32)dp * 128u + (u32)(l & 7) * 4u), "s"(ws) : "memory");
    WAIT_L0();
    BAR();
    if (rg < 2) {
      f32x4 sp = rg == 0 ? kvt[0] : kvt[2], sn = rg == 0 ? kvt[1] : kvt[3];
#pragma unroll
      for (int sl = 0; sl < 3; ++sl) {
        sp += lds128f(L, L_SCR + (u32)((((cg * 2 + rg) * 2 + 0) * 3 + sl) * 1024 + l * 16));
        sn += lds128f(L, L_SCR + (u32)((((cg * 2 + rg) * 2 + 1) * 3 + sl) * 1024 + l * 16));
      }
      u32x4 o = {pk2(sp[0], sp[1]), pk2(sp[2], sp[3]), pk2(sn[0], sn[1]), pk2(sn[2], sn[3])};
      st_sc1_x4(ws, WS_KVP + (u32)(((g * 16 + p) * 4 + 2 * cg + rg) * 1024 + l * 16), o);
    } else if (l < 8) {
      const int pi = rg - 2, sg = l >> 2, qq = l & 3;
      f32x4 s = {0, 0, 0, 0};
#pragma unroll
      for (int r = 0; r < 4; ++r) s += lds128f(L, L_ZR + (u32)(((((cg * 2 + pi) * 2 + sg) * 4 + r) * 16 + 4 * qq) * 4));
      st_sc1_x4(ws, WS_ZP + (u32)((((g * 4 + 2 * cg + pi) * 16 + p) * 32 + l * 4) * 4), __builtin_bit_cast(u32x4, s));
    }
#pragma unroll
    for (int rt = 0; rt < 2; ++rt) phq[0][rt] = dual_softmax(logits(0, 0, rt));
    asm volatile("s_waitcnt vmcnt(0)" : "+v"(hv) :: "memory");
    if (w == 0 && __builtin_amdgcn_ballot_w64(hv != tag) != 0ull) poll_flags(ws, WS_HFLAG + (u32)dp * 128u, 8, l, tag);
    BAR();
    if (threadIdx.x == 0) st_sc1_x1(ws, WS_KVFLAG + (u32)g * 128u + (u32)p * 4u, tag);
    STAMP(4);
    loadA(0); loadA(1); loadA(2); loadA(3);
#pragma unroll
    for (int rt = 0; rt < 2; ++rt) phq[1][rt] = dual_softmax(logits(0, 1, rt));
    if (w == 0) poll_flags(ws, WS_KVFLAG + (u32)g * 128u, 16, l, tag);
    BAR();
    STAMP(5);
    {
      const int hd = w & 3, hf = w >> 2;
#pragma unroll
      for (int i = 0; i < 8; ++i) ld_sc1_x4(gat[i], ws, WS_KVP + (u32)(((g * 16 + 8 * hf + i) * 4 + hd) * 1024 + l * 16));
      ld_sc1_x4(zg, ws, WS_ZP + (u32)(((g * 4 + hd) * 16 + 8 * hf) * 128 + l * 16));
      BAR();
      loadA(4); loadA(5);
      asm volatile("s_waitcnt vmcnt(8)" : "+v"(gat[0]), "+v"(gat[1]), "+v"(gat[2]), "+v"(gat[3]), "+v"(gat[4]), "+v"(gat[5]),
                   "+v"(gat[6]), "+v"(gat[7]), "+v"(zg) :: "memory");
    }
    STAMP(6);
    {
      const int hd = w & 3, hf = w >> 2;
      f32x4 s0 = {0, 0, 0, 0}, s1 = {0, 0, 0, 0};
#pragma unroll
      for (int i = 0; i < 8; ++i) {
        s0[0] += bflo(gat[i][0]); s0[1] += bfhi(gat[i][0]); s0[2] += bflo(gat[i][1]); s0[3] += bfhi(gat[i][1]);
        s1[0] += bflo(gat[i][2]); s1[1] += bfhi(gat[i][2]); s1[2] += bflo(gat[i][3]); s1[3] += bfhi(gat[i][3]);
      }
      *(AS3 f32x4*)(L + L_SCR + (u32)((hd * 2 + hf) * 2048 + l * 32)) = s0;
      *(AS3 f32x4*)(L + L_SCR + (u32)((hd * 2 + hf) * 2048 + l * 32 + 16)) = s1;
      f32x4 zs = __builtin_bit_cast(f32x4, zg);
#pragma unroll
      for (int r = 0; r < 4; ++r) {
        float v = zs[r];
        v += __shfl_xor(v, 8); v += __shfl_xor(v, 16); v += __shfl_xor(v, 32);
        zs[r] = v;
      }
      if (l < 8) *(AS3 f32x4*)(L + L_ZR + (u32)(((hd * 2 + hf) * 32 + l * 4) * 4)) = zs;
    }
    WAIT_L0();
    BAR();
    {
      u32x4 o0, o1;
#pragma unroll
      for (int pi = 0; pi < 2; ++pi) {
        const int hd = 2 * cg + pi;
        const u32 ga = L_SCR + (u32)(hd * 4096 + l * 32);
        const f32x4 kp = lds128f(L, ga) + lds128f(L, ga + 2048u), kn = lds128f(L, ga + 16u) + lds128f(L, ga + 2064u);
        const u32 za = L_ZR + (u32)((hd * 64 + 4 * gq) * 4);
        const f32x4 zp = lds128f(L, za) + lds128f(L, za + 128u), zn = lds128f(L, za + 64u) + lds128f(L, za + 192u);
        const u32x4 kf = {pk2(kp[0], kp[1]), pk2(kp[2], kp[3]), pk2(kn[0], kn[1]), pk2(kn[2], kn[3])};
        const u32x4 zf = {pk2(zp[0], zp[1]), pk2(zp[2], zp[3]), pk2(zn[0], zn[1]), pk2(zn[2], zn[3])};
#pragma unroll
        for (int rt = 0; rt < 2; ++rt) {
          const f32x4 num = mfma32(kf, phq[pi][rt], zero4), den = mfma32(zf, phq[pi][rt], zero4);
          const float rd = __builtin_amdgcn_rcpf(den[0] + 1e-6f);
          const u32 a = pk2(num[0] * rd, num[1] * rd), bq = pk2(num[2] * rd, num[3] * rd);
          if (rt == 0) { o0[2 * pi] = a; o0[2 * pi + 1] = bq; } else { o1[2 * pi] = a; o1[2 * pi + 1] = bq; }
        }
      }
      const u32 off = WS_AF + ((u32)(8 * p + 2 * rg) * 72u + (u32)(56 + 2 * g + cg)) * 1024u + (u32)l * 16u;
      waitA05();
      st_sc1_x4(ws, off, o0);
      st_sc1_x4(ws, off + 72u * 1024u, o1);
    }
    BAR();
    STAMP(7);
  }
  {
    u32x4 xres, b2v;
    const u32 fo = (u32)(64 * rb + (threadIdx.x >> 3)) * 1024u + (u32)(cb * 128 + (threadIdx.x & 7) * 16);
    f32x4 dacc[4][4];
#pragma unroll
    for (int i = 0; i < 4; ++i)
#pragma unroll
      for (int j = 0; j < 4; ++j) dacc[i][j] = f32x4{0, 0, 0, 0};
    auto waitA = [&](int j, auto n) { wait4<decltype(n)::value>(a[j][0], a[j][1], a[j][2], a[j][3]); };
    auto step = [&](int j) {
#pragma unroll
      for (int ct = 0; ct < 4; ++ct) {
        const u32x4 bf = cat(blo[j & 1][ct], bhi[j & 1][ct]);
#pragma unroll
        for (int rt = 0; rt < 4; ++rt) dacc[rt][ct] = mfma32(bf, a[j][rt], dacc[rt][ct]);
      }
      SCHED();
    };
    step(0); rdB(IC<2>{});
    step(1); waitb(blo[0], bhi[0]); rdB(IC<3>{}); issueB(6);
    WAIT_V(4);
    if (typeA && l == 0) st_sc1_x1(ws, WS_OFLAG + (u32)((p * 8 + g) * 8 + w) * 4u, tag);
    STAMP(8);
    loadA(6);
    step(2); waitb(blo[1], bhi[1]); rdB(IC<4>{}); issueB(7);
    step(3); waitb(blo[0], bhi[0]); rdB(IC<5>{}); issueB(8);
    step(4); waitb(blo[1], bhi[1]); WAIT_V(12); rdB(IC<6>{});
    step(5); waitb(blo[0], bhi[0]); waitA(6, IC<4>{}); rdB(IC<7>{});
    step(6); waitb(blo[1], bhi[1]);
    STAMP(9);
    if (w == 0) {
      poll_o(ws, WS_OFLAG + (u32)((dp * 8 + ((l >> 2) & 7)) * 8 + 2 * (rb & 1) + (l & 1) + 4 * ((l >> 1) & 1)) * 4u, tag);
      *(volatile AS3 u32*)(L + L_OFL) = tag;
    } else {
#pragma unroll 1
      for (int it = 0; it < SPIN_MAX * 8; ++it) {
        if ((u32)__builtin_amdgcn_readfirstlane((int)*(volatile AS3 u32*)(L + L_OFL)) == tag) break;
        __builtin_amdgcn_s_sleep(2);
      }
    }
    STAMP(10);
    loadA(7); loadA(8);
    ld_x4(xres, x, fo);
    ld_x4(b2v, b2, (u32)(cb * 128 + (threadIdx.x & 7) * 16));
    WAIT_V(10); rdB(IC<8>{});
    STAMP(13);
    waitA(7, IC<6>{}); step(7);
    waitA(8, IC<2>{}); waitb(blo[0], bhi[0]); step(8);
    STAMP(14);
    WAIT_L0();
#pragma unroll
    for (int rt = 0; rt < 4; ++rt)
#pragma unroll
      for (int ct = 0; ct < 4; ++ct)
        *(AS3 f32x4*)(L + dslot(rt) + (u32)(r16 * 256 + (((ct * 4 + gq) ^ r16) * 16))) = dacc[rt][ct];
    WAIT_L0();
    BAR();
    STAMP(11);
    {
      const int row = threadIdx.x >> 3, cq = threadIdx.x & 7, rt = row >> 4, tk = row & 15;
      const u32 pb = (u32)((rt + 3) & 3) * 32768u + (u32)(tk * 256);
      f32x4 s0 = {0, 0, 0, 0}, s1 = {0, 0, 0, 0};
#pragma unroll
      for (int ww = 0; ww < 8; ++ww) {
        s0 += lds128f(L, pb + (u32)(ww * 4096) + (u32)(((2 * cq) ^ tk) * 16));
        s1 += lds128f(L, pb + (u32)(ww * 4096) + (u32)(((2 * cq + 1) ^ tk) * 16));
      }
      asm volatile("s_waitcnt vmcnt(0)" : "+v"(xres), "+v"(b2v) :: "memory");
      u32x4 o;
      o[0] = pk2(s0[0] + bflo(xres[0]) + bflo(b2v[0]), s0[1] + bfhi(xres[0]) + bfhi(b2v[0]));
      o[1] = pk2(s0[2] + bflo(xres[1]) + bflo(b2v[1]), s0[3] + bfhi(xres[1]) + bfhi(b2v[1]));
      o[2] = pk2(s1[0] + bflo(xres[2]) + bflo(b2v[2]), s1[1] + bfhi(xres[2]) + bfhi(b2v[2]));
      o[3] = pk2(s1[2] + bflo(xres[3]) + bflo(b2v[3]), s1[3] + bfhi(xres[3]) + bfhi(b2v[3]));
      *(u32x4*)(out + fo) = o;
    }
    if (b == 255 && w == 7) {
#pragma unroll 1
      for (int i = 0; i < 4; ++i) poll_flags(ws, WS_AFLAG + (u32)i * 256u, 64, l, tag);
      if (l == 0) st_sc1_x1(ws, WS_EPOCH, tag);
    }
    STAMP(12);
    STAMP_FLUSH;
  }
}

extern "C" void kernel_launch(void* const* d_in, const int* in_sizes, int n_in, void* d_out, int out_size, void* d_ws,
                              size_t ws_size, hipStream_t stream) {
  (void)in_sizes; (void)n_in; (void)out_size; (void)ws_size;
  hipFuncSetAttribute((const void*)fused_kernel, hipFuncAttributeMaxDynamicSharedMemorySize, LDS_BYTES);
  fused_kernel<<<256, 512, LDS_BYTES, stream>>>((const char*)d_in[0], (const char*)d_in[1], (const char*)d_in[2],
                                                (const char*)d_in[3], (const char*)d_in[5], (const char*)d_in[6],
                                                (const char*)d_in[7], (const char*)d_in[8], (char*)d_out, (char*)d_ws);
}
```

```cpp
#include <hip/hip_runtime.h>
#include <stdint.h>

#define EXP_NODMA 0
#define EXP_NOBAR 0
typedef unsigned int u32;
typedef unsigned long long u64;
typedef __bf16 bf16x8 __attribute__((ext_vector_type(8)));
typedef __bf16 bf16x2 __attribute__((ext_vector_type(2)));
typedef short v4i16 __attribute__((ext_vector_type(4)));
typedef float f32x4 __attribute__((ext_vector_type(4)));
typedef float f32x2 __attribute__((ext_vector_type(2)));
typedef u32 u32x4 __attribute__((ext_vector_type(4)));
typedef u32 u32x2 __attribute__((ext_vector_type(2)));

#define AS1 __attribute__((address_space(1)))
#define AS3 __attribute__((address_space(3)))
#define AS4 __attribute__((address_space(4)))
#define DEVI __device__ __forceinline__

constexpr u32 WS_EPOCH = 0;
constexpr u32 WS_AFLAG = 1024;
constexpr u32 WS_HFLAG = 2048;
constexpr u32 WS_OFLAG = 4096;
constexpr u32 WS_STAT = 8192;
constexpr u32 WS_CS = 40960;
constexpr u32 WS_KVFLAG = 0x4A000;
constexpr u32 WS_KVP = 0x50000;
constexpr u32 WS_ZP = 0xD0000;
constexpr u32 WS_AF = 0x100000;
constexpr u32 WS_TIM = 0xB00000;
constexpr u32 WS_USED = 0xC00000;

constexpr u32 SLOT = 49152;
constexpr u32 L_SCR = 131072;
constexpr u32 L_ZR = L_SCR + 24576;
constexpr u32 L_CSB = 157696;
constexpr u32 L_MEAN = 159744;
constexpr u32 L_RSTD = 160256;
constexpr u32 L_WPHI = 160768;
constexpr u32 L_PF = 162816;
constexpr u32 L_OFL = 163072;
constexpr u32 LDS_BYTES = 163328;
constexpr u32 CODE_LINES = 296;

DEVI f32x4 mfma32(u32x4 a, u32x4 b, f32x4 c) {
  return __builtin_amdgcn_mfma_f32_16x16x32_bf16(__builtin_bit_cast(bf16x8, a), __builtin_bit_cast(bf16x8, b), c, 0, 0, 0);
}
DEVI f32x4 mfma16(u32x2 a, u32x2 b, f32x4 c) {
  return __builtin_amdgcn_mfma_f32_16x16x16bf16_1k(__builtin_bit_cast(v4i16, a), __builtin_bit_cast(v4i16, b), c, 0, 0, 0);
}
DEVI u32 pk2(float a, float b) {
  f32x2 v = {a, b};
  return __builtin_bit_cast(u32, __builtin_convertvector(v, bf16x2));
}
DEVI float bflo(u32 v) { return __builtin_bit_cast(float, v << 16); }
DEVI float bfhi(u32 v) { return __builtin_bit_cast(float, v & 0xffff0000u); }
DEVI u32 fbits(float f) { return __builtin_bit_cast(u32, f); }
DEVI float bitsf(u32 u) { return __builtin_bit_cast(float, u); }

DEVI u32x2 lds_tr(AS3 char* L, u32 addr) {
  v4i16 t = __builtin_amdgcn_ds_read_tr16_b64_v4i16((AS3 v4i16*)(L + addr));
  return __builtin_bit_cast(u32x2, t);
}
DEVI u32x4 lds_frag_tr(AS3 char* L, u32 addr, u32 second) {
  u32x2 a = lds_tr(L, addr), b = lds_tr(L, addr + second);
  u32x4 r = {a[0], a[1], b[0], b[1]};
  return r;
}
DEVI u32x4 lds128(AS3 char* L, u32 addr) { return *(AS3 u32x4*)(L + addr); }
DEVI f32x4 lds128f(AS3 char* L, u32 addr) { return *(AS3 f32x4*)(L + addr); }
template <int OFF> DEVI void glds_m0(u32 m0v, const char* sbase, u32 voff) {
  asm volatile("s_mov_b32 m0, %0\n\ts_nop 0\n\tglobal_load_lds_dwordx4 %1, %2 offset:%3" ::"s"(m0v), "v"(voff), "s"(sbase), "n"(OFF) : "memory");
}
template <int OFF> DEVI void glds(const char* sbase, u32 voff) {
  asm volatile("global_load_lds_dwordx4 %0, %1 offset:%2" ::"v"(voff), "s"(sbase), "n"(OFF) : "memory");
}
template <int OFF> DEVI void glds_masked(const char* sbase, u32 voff, u64 mask) {
  asm volatile("s_mov_b64 exec, %3\n\tglobal_load_lds_dwordx4 %0, %1 offset:%2\n\ts_mov_b64 exec, -1" ::"v"(voff), "s"(sbase), "n"(OFF), "s"(mask) : "memory");
}
DEVI void pf_lds(u32 m0v, const char* sbase, u32 voff, u64 mask) {
  asm volatile("s_mov_b32 m0, %0\n\ts_mov_b64 exec, %3\n\tglobal_load_lds_dword %1, %2\n\ts_mov_b64 exec, -1" ::"s"(m0v), "v"(voff), "s"(sbase), "s"(mask) : "memory");
}
template <int OFF> DEVI void dsr128(u32x4& d, u32 a) {
  asm volatile("ds_read_b128 %0, %1 offset:%2" : "=v"(d) : "v"(a), "n"(OFF) : "memory");
}
template <int OFF> DEVI void dsrtr(u32x2& d, u32 a) {
  asm volatile("ds_read_b64_tr_b16 %0, %1 offset:%2" : "=v"(d) : "v"(a), "n"(OFF) : "memory");
}
DEVI u32x4 cat(u32x2 a, u32x2 b) { u32x4 r = {a[0], a[1], b[0], b[1]}; return r; }
#define WAIT_V(n) asm volatile("s_waitcnt vmcnt(%0)" ::"n"(n) : "memory")
#define WAIT_L0() asm volatile("s_waitcnt lgkmcnt(0)" ::: "memory")
#define BAR() __builtin_amdgcn_s_barrier()
#define SCHED() __builtin_amdgcn_sched_barrier(0)

DEVI void ld_sc1_x4(u32x4& d, const char* base, u32 off) {
  asm volatile("global_load_dwordx4 %0, %1, %2 sc1" : "=v"(d) : "v"(off), "s"(base) : "memory");
}
DEVI void ld_x4(u32x4& d, const char* base, u32 off) {
  asm volatile("global_load_dwordx4 %0, %1, %2" : "=v"(d) : "v"(off), "s"(base) : "memory");
}
DEVI void ld_x1(u32& d, const char* base, u32 off) {
  asm volatile("global_load_dword %0, %1, %2" : "=v"(d) : "v"(off), "s"(base) : "memory");
}
DEVI void ld_u16(u32& d, const char* base, u32 off) {
  asm volatile("global_load_ushort %0, %1, %2" : "=v"(d) : "v"(off), "s"(base) : "memory");
}
DEVI u32 ld_sc1_x1_wait(const char* base, u32 off) {
  u32 d;
  asm volatile("global_load_dword %0, %1, %2 sc1\n\ts_waitcnt vmcnt(0)" : "=v"(d) : "v"(off), "s"(base) : "memory");
  return d;
}
DEVI void st_sc1_x4(char* base, u32 off, u32x4 v) {
  asm volatile("global_store_dwordx4 %0, %1, %2 sc1\n\ts_nop 1" ::"v"(off), "v"(v), "s"(base) : "memory");
}
DEVI void st_sc1_x2(char* base, u32 off, u32x2 v) {
  asm volatile("global_store_dwordx2 %0, %1, %2 sc1" ::"v"(off), "v"(v), "s"(base) : "memory");
}
DEVI void st_sc1_x1(char* base, u32 off, u32 v) {
  asm volatile("global_store_dword %0, %1, %2 sc1" ::"v"(off), "v"(v), "s"(base) : "memory");
}
constexpr int SPIN_MAX = 1 << 17;
DEVI void poll_flags(const char* base, u32 off0, int n, int l, u32 tag) {
  u32 off = off0 + (u32)(l < n ? l : 0) * 4u;
#pragma unroll 1
  for (int it = 0; it < SPIN_MAX; ++it) {
    u32 v = ld_sc1_x1_wait(base, off);
    if (__builtin_amdgcn_ballot_w64(v != tag) == 0ull) break;
    __builtin_amdgcn_s_sleep(1);
  }
  __builtin_amdgcn_fence(__ATOMIC_ACQUIRE, "wavefront");
}

#define POLL_SLEEP 1
DEVI void poll_o(const char* base, u32 off, u32 tag) {
#pragma unroll 1
  for (int it = 0; it < SPIN_MAX; ++it) {
    u32 v = ld_sc1_x1_wait(base, off);
    if (__builtin_amdgcn_ballot_w64(v != tag) == 0ull) break;
    __builtin_amdgcn_s_sleep(POLL_SLEEP);
  }
  __builtin_amdgcn_fence(__ATOMIC_ACQUIRE, "wavefront");
}

#define STAMP(i) do {} while (0)
#define STAMPG(i) do {} while (0)
#define STAMP_DECL do {} while (0)
#define STAMP_FLUSH do {} while (0)

DEVI void red4_max2(float& a, float& b) {
  auto s = __builtin_amdgcn_permlane32_swap(fbits(a), fbits(b), false, false);
  float t = fmaxf(bitsf(s[0]), bitsf(s[1]));
  auto u = __builtin_amdgcn_permlane16_swap(fbits(t), fbits(t), false, false);
  float m = fmaxf(bitsf(u[0]), bitsf(u[1]));
  auto z = __builtin_amdgcn_permlane32_swap(fbits(m), fbits(m), false, false);
  a = bitsf(z[0]); b = bitsf(z[1]);
}
DEVI void red4_sum2_rcp(float& a, float& b) {
  auto s = __builtin_amdgcn_permlane32_swap(fbits(a), fbits(b), false, false);
  float t = bitsf(s[0]) + bitsf(s[1]);
  auto u = __builtin_amdgcn_permlane16_swap(fbits(t), fbits(t), false, false);
  float m = __builtin_amdgcn_rcpf(bitsf(u[0]) + bitsf(u[1]));
  auto z = __builtin_amdgcn_permlane32_swap(fbits(m), fbits(m), false, false);
  a = bitsf(z[0]); b = bitsf(z[1]);
}
DEVI u32x4 dual_softmax(f32x4 p) {
  const float L2E = 1.4426950408889634f;
  float mx = fmaxf(fmaxf(p[0], p[1]), fmaxf(p[2], p[3]));
  float nm = fmaxf(fmaxf(-p[0], -p[1]), fmaxf(-p[2], -p[3]));
  red4_max2(mx, nm);
  float a = -mx * L2E, c = -nm * L2E;
  float e0 = __builtin_amdgcn_exp2f(fmaf(p[0], L2E, a)), e1 = __builtin_amdgcn_exp2f(fmaf(p[1], L2E, a));
  float e2 = __builtin_amdgcn_exp2f(fmaf(p[2], L2E, a)), e3 = __builtin_amdgcn_exp2f(fmaf(p[3], L2E, a));
  float n0 = __builtin_amdgcn_exp2f(fmaf(p[0], -L2E, c)), n1 = __builtin_amdgcn_exp2f(fmaf(p[1], -L2E, c));
  float n2 = __builtin_amdgcn_exp2f(fmaf(p[2], -L2E, c)), n3 = __builtin_amdgcn_exp2f(fmaf(p[3], -L2E, c));
  float sp = (e0 + e1) + (e2 + e3), sn = (n0 + n1) + (n2 + n3);
  red4_sum2_rcp(sp, sn);
  u32x4 r = {pk2(e0 * sp, e1 * sp), pk2(e2 * sp, e3 * sp), pk2(n0 * sn, n1 * sn), pk2(n2 * sn, n3 * sn)};
  return r;
}

#define PF_MODE 4
constexpr int PFN = PF_MODE == 1 ? 2 : 0;
template <int V> struct IC { static constexpr int value = V; };
template <int N> DEVI void wait4(u32x4& a, u32x4& b, u32x4& c, u32x4& d) {
  asm volatile("s_waitcnt vmcnt(%4)" : "+v"(a), "+v"(b), "+v"(c), "+v"(d) : "n"(N) : "memory");
}
template <int N> DEVI void wait1(u32x4& a) { asm volatile("s_waitcnt vmcnt(%1)" : "+v"(a) : "n"(N) : "memory"); }

template <int N> DEVI void waitset(u32x4& a, u32x4& b, u32x2 (&lo)[8], u32x2 (&hi)[8]);
template <> DEVI void waitset<8>(u32x4& a, u32x4& b, u32x2 (&lo)[8], u32x2 (&hi)[8]) {
  asm volatile("s_waitcnt lgkmcnt(0)"
               : "+v"(a), "+v"(b), "+v"(lo[0]), "+v"(lo[1]), "+v"(lo[2]), "+v"(lo[3]), "+v"(lo[4]), "+v"(lo[5]), "+v"(lo[6]), "+v"(lo[7]),
                 "+v"(hi[0]), "+v"(hi[1]), "+v"(hi[2]), "+v"(hi[3]), "+v"(hi[4]), "+v"(hi[5]), "+v"(hi[6]), "+v"(hi[7])
               :: "memory");
}
template <> DEVI void waitset<6>(u32x4& a, u32x4& b, u32x2 (&lo)[8], u32x2 (&hi)[8]) {
  asm volatile("s_waitcnt lgkmcnt(0)"
               : "+v"(a), "+v"(b), "+v"(lo[0]), "+v"(lo[1]), "+v"(lo[2]), "+v"(lo[3]), "+v"(lo[4]), "+v"(lo[5]),
                 "+v"(hi[0]), "+v"(hi[1]), "+v"(hi[2]), "+v"(hi[3]), "+v"(hi[4]), "+v"(hi[5])
               :: "memory");
}
DEVI void waitb(u32x2 (&lo)[4], u32x2 (&hi)[4]) {
  asm volatile("s_waitcnt lgkmcnt(0)"
               : "+v"(lo[0]), "+v"(lo[1]), "+v"(lo[2]), "+v"(lo[3]), "+v"(hi[0]), "+v"(hi[1]), "+v"(hi[2]), "+v"(hi[3]) :: "memory");
}
DEVI float wave_sum(float v) {
#define DPP_ADD(ctrl, rmask) v += bitsf((u32)__builtin_amdgcn_update_dpp(0, (int)fbits(v), ctrl, rmask, 0xf, false))
  DPP_ADD(0x111, 0xf); DPP_ADD(0x112, 0xf); DPP_ADD(0x114, 0xf); DPP_ADD(0x118, 0xf);
  DPP_ADD(0x142, 0xa); DPP_ADD(0x143, 0xc);
#undef DPP_ADD
  return bitsf((u32)__builtin_amdgcn_readlane((int)fbits(v), 63));
}

extern "C" __global__ void __launch_bounds__(512, 2)
fused_kernel(const char* __restrict__ x, const char* __restrict__ wqkv, const char* __restrict__ wphi,
             const char* __restrict__ wo, const char* __restrict__ w1, const char* __restrict__ b1,
             const char* __restrict__ w2, const char* __restrict__ b2, char* __restrict__ out, char* __restrict__ ws) {
  extern __shared__ __attribute__((aligned(16))) char smem[];
  AS3 char* L = (AS3 char*)smem;
  const int b = blockIdx.x;
  const int l = threadIdx.x & 63;
  const int w = __builtin_amdgcn_readfirstlane(threadIdx.x >> 6);
  u32 pfd;
  {
    u64 pc;
    asm volatile("s_getpc_b64 %0" : "=s"(pc));
    const u32 ln = (u32)(w * 40 + (l < 40 ? l : 39));
    ld_x1(pfd, (const char*)(pc & ~127ull), (ln < CODE_LINES ? ln : CODE_LINES - 1u) * 128u);
  }
  asm volatile("" ::"s"(x), "s"(wqkv), "s"(wphi), "s"(wo), "s"(w1), "s"(b1), "s"(w2), "s"(b2), "s"(out), "s"(ws));
  const int xcd = b & 7, bi = b >> 3;
  const int p = (xcd >> 2) * 8 + (bi >> 2), c = (xcd & 3) * 4 + (bi & 3), g = c >> 1;
  const bool typeA = (c & 1) == 0;
  const int rg = w & 3, cg = w >> 2;
  const int gq = l >> 4, r16 = l & 15, q4 = (l >> 2) & 3, pp = l & 3;
  const int rot = (p & 1) * 4;
  const int rb = 4 * xcd + (bi >> 3), cb = bi & 7;
  STAMP_DECL;
  STAMP(0);

  const u32 lb = (u32)(size_t)L;
  const u32 xrow_off = (u32)(128 * p + 8 * c + w) * 1024u + (u32)l * 16u;
  u32 gb1[2];
  const u32 gb1_off = (u32)(224 * g + (w < 6 ? 32 * w + ((g & 1) ? 32 : 0) : (w == 6 && !(g & 1) ? 192 : 0)) + (l >> 2)) * 2u;
  u32 tag;

  const char* xsrc = x + (size_t)p * 131072u;
  const u32 wstride = typeA ? 3072u : 3584u;
  const char* wsrc = (typeA ? (wqkv + g * 128) : (w1 + 448 * g)) - 4096;
  const u32 vx0 = (u32)(16 * w + (l >> 3)) * 1024u + (u32)(((l & 7) ^ (l >> 4)) * 16);
  const u32 vx1 = (u32)(16 * w + 7 + (l >> 3)) * 1024u + (u32)(((l & 7) ^ (4 + (l >> 4))) * 16);
  const u32 wsp = (u32)(((l & 7) >> 1) ^ (((l >> 4) & 1) | ((w & 1) << 1)));
  const u32 vw = (u32)(8 * w + (l >> 3)) * wstride + (wsp * 2u + (u32)(l & 1)) * 16u + 4096u;
  u32 vws[4];
  {
    const u32 odd = (u32)(g & 1) * 64u;
    vws[0] = typeA ? vw : vw + odd;
    vws[1] = typeA ? vw : vw + odd + 128u - 1024u;
    vws[2] = typeA ? vw : vw + odd + 256u - 2048u;
    vws[3] = vw + (odd ? 0u : 384u) - 3072u;
  }
  const u64 hmask = __builtin_amdgcn_ballot_w64(wsp < 2u);
  const u32 m0x = lb + (u32)w * 2048u, m0w = lb + 16384u + (u32)w * 4096u;

  const char* dsrc2 = w2 + (size_t)(7 * w) * 32768u + cb * 128;
  const char* dsrco = wo + (size_t)(2 * w) * 32768u + cb * 128;
  const u32 vdb = (u32)(l >> 3) * 1024u + (u32)(((((l & 7) >> 1) ^ ((l >> 4) & 3)) * 2 + (l & 1)) * 16);
  auto dslot = [&](int j) -> u32 { return (u32)((j + 3) & 3) * 32768u + (u32)w * 4096u; };
  auto issueB = [&](int j) {
    const char* s = (j < 7) ? (dsrc2 + j * 32768) : (dsrco + (j - 7) * 32768);
    glds_m0<0>((u32)__builtin_amdgcn_readfirstlane((int)(lb + dslot(j))), s, vdb);
    glds<1024>(s, vdb + 7168u);
    glds<2048>(s, vdb + 2u * 7168u);
    glds<3072>(s, vdb + 3u * 7168u);
  };

  auto issueBk = [&](int j, int k) {
    const char* s = (j < 7) ? (dsrc2 + j * 32768) : (dsrco + (j - 7) * 32768);
    if (k == 0) glds_m0<0>(lb + dslot(j), s, vdb);
    if (k == 1) glds<1024>(s, vdb + 7168u);
    if (k == 2) glds<2048>(s, vdb + 2u * 7168u);
    if (k == 3) glds<3072>(s, vdb + 3u * 7168u);
  };

  auto prefetchW = [&]() {
    const u32 wi = (u32)((bi >> 1) * 8 + w);
    const u32 row = 112u * (wi >> 4) + 64u + 3u * (wi & 15u) + (u32)(l < 48 ? l >> 4 : 0);
    pf_lds(lb + L_PF, w2, row * 2048u + (u32)(l & 15) * 128u, 0x0000ffffffffffffull);
    pf_lds(lb + L_PF, wo, (wi * 4u) * 1024u + (u32)(l & 31) * 128u, 0xffffffffull);
  };
  u32 xa0 = lb + (u32)(32 * rg + r16) * 128u + (u32)((gq ^ ((r16 >> 1) & 7)) * 16);
  u32 xa1 = xa0 ^ 64u;
  const u32 vbase = lb + 16384u + (u32)gq * 4096u + (u32)q4 * 128u + (u32)pp * 8u + (u32)(((q4 >> 1) & 1) | ((gq & 1) << 1)) * 32u;
  u32 R[8];
#pragma unroll
  for (int t = 0; t < 8; ++t) {
    const u32 ra = (vbase ^ (u32)((2 * cg + (t & 1)) * 32)) + (u32)(t >> 1) * 1024u;
    const u32 rbb = (vbase ^ (u32)((t & 3) * 32)) + (u32)(t >> 2) * 1024u + (u32)cg * 2048u;
    R[t] = typeA ? ra : rbb;
  }

  f32x4 acc[8][2];
#pragma unroll
  for (int t = 0; t < 8; ++t) { acc[t][0] = f32x4{0, 0, 0, 0}; acc[t][1] = f32x4{0, 0, 0, 0}; }
  u32x4 gat[8];
  const u32x4 ones = {0x3F803F80u, 0x3F803F80u, 0x3F803F80u, 0x3F803F80u};
  const u32 gcs = WS_CS + (u32)(c * 256 + 32 * w + (l >> 2)) * 64u + (u32)(l & 3) * 16u;
  const u32 gst = WS_STAT + (u32)(128 * p + 16 * w + r16) * 16u;

  auto g1 = [&](auto TAc, auto N8c) {
    constexpr bool TA = decltype(TAc)::value != 0, N8 = decltype(N8c)::value != 0;
    constexpr int NG = TA ? 5 : 6;
    u32x4 fx[2][2];
    u32x2 flo[2][8], fhi[2][8];
    u32x4 xrow;
    u32 epoch;
    asm volatile("s_load_dword %0, %1, 0x0" : "=s"(epoch) : "s"(ws) : "memory");
    ld_x4(xrow, x, xrow_off);
    if (TA) { gb1[0] = 0; gb1[1] = 0; }
    else { ld_u16(gb1[0], b1, gb1_off); ld_u16(gb1[1], b1, gb1_off + 32u); }
    auto issue = [&](int s, u32 sb) {
      const int ks = (s + rot) & 7;
      const char* xs = xsrc + ks * 128;
      const char* wsb = wsrc + (size_t)((u32)ks * 64u * wstride);
      glds_m0<0>(m0x + sb, xs, vx0);
      glds<1024>(xs, vx1);
      glds_m0<0>(m0w + sb, wsb, vws[0]);
      glds<1024>(wsb, vws[1]);
      glds<2048>(wsb, vws[2]);
      if (!TA) glds_masked<3072>(wsb, vws[3], hmask);
    };
    issue(0, 0); issue(1, SLOT); issue(2, 2 * SLOT);
    if (TA && w < 2) glds_m0<0>(lb + L_WPHI + (u32)w * 1024u, wphi + g * 2048 + w * 1024, (u32)l * 16u);

#define MF(S, t, rt) acc[t][rt] = mfma32(cat(flo[S][t], fhi[S][t]), fx[S][rt], acc[t][rt])
#define MFS(S, t, rt) acc[t][rt] = mfma32(fx[S][rt], cat(flo[S][t], fhi[S][t]), acc[t][rt])
#define RW(N, t) do { if (RD) { dsrtr<(N) * 16384>(flo[N][t], R[t]); dsrtr<(N) * 16384 + 512>(fhi[N][t], R[t]); } } while (0)
#define RX(N) do { if (RD) { dsr128<0>(fx[N][0], (N) ? xa1 : xa0); dsr128<2048>(fx[N][1], (N) ? xa1 : xa0); } } while (0)
    auto waitS = [&](auto Sc) {
      constexpr int S = decltype(Sc)::value;
      if constexpr (N8) waitset<8>(fx[S][0], fx[S][1], flo[S], fhi[S]);
      else waitset<6>(fx[S][0], fx[S][1], flo[S], fhi[S]);
    };
    auto phase = [&](auto Sc, auto RDc, auto GLc, const char* xs, const char* wsb, u32 sb) {
      constexpr int S = decltype(Sc)::value, N = 1 - S;
      constexpr bool RD = decltype(RDc)::value != 0;
      constexpr int GL = decltype(GLc)::value;
      waitS(Sc);
      if (S == 1 && RD && !EXP_NOBAR) BAR();
      SCHED();
#define DM(i) do { if (EXP_NODMA) break; \
        if (GL == 1 && (i) == 0) glds_m0<0>(m0x + sb, xs, vx0); \
        if (GL == 1 && (i) == 1) glds<1024>(xs, vx1); \
        if (GL == 1 && (i) == 2) glds_m0<0>(m0w + sb, wsb, vws[0]); \
        if (GL == 2 && (i) == 0) glds_m0<1024>(m0w + sb, wsb, vws[1]); \
        if (GL == 2 && (i) == 1) glds<2048>(wsb, vws[2]); \
        if (GL == 2 && (i) == 2 && !TA) glds_masked<3072>(wsb, vws[3], hmask); } while (0)
      if constexpr (N8) {
        MF(S, 6, 0); RW(N, 6); SCHED();
        MF(S, 6, 1); RW(N, 7); SCHED();
        MF(S, 7, 0); DM(0); SCHED();
        MF(S, 7, 1); SCHED();
      }
      if constexpr (TA) {
        MFS(S, 4, 0); RX(N); SCHED();
        MFS(S, 4, 1); RW(N, 4); SCHED();
        MFS(S, 5, 0); if (!N8) DM(0); SCHED();
        MFS(S, 5, 1); RW(N, 5); SCHED();
      } else {
        MF(S, 4, 0); RX(N); SCHED();
        MF(S, 4, 1); RW(N, 4); SCHED();
        MF(S, 5, 0); if (!N8) DM(0); SCHED();
        MF(S, 5, 1); RW(N, 5); SCHED();
      }
      MF(S, 0, 0); RW(N, 0); SCHED();
      MF(S, 0, 1); RW(N, 1); SCHED();
      MF(S, 1, 0); DM(1); SCHED();
      MF(S, 1, 1); RW(N, 2); SCHED();
      MF(S, 2, 0); RW(N, 3); SCHED();
      MF(S, 2, 1); SCHED();
      MF(S, 3, 0); DM(2); SCHED();
      MF(S, 3, 1); SCHED();
#undef DM
    };
    auto advance = [&](u32 d) {
#pragma unroll
      for (int t = 0; t < (N8 ? 8 : 6); ++t) R[t] += d;
      xa0 += d; xa1 += d;
    };

    STAMP(1);
    if (TA) asm volatile("s_waitcnt vmcnt(%1)" : "+v"(xrow) : "n"(2 * NG) : "memory");
    else asm volatile("s_waitcnt vmcnt(%3)" : "+v"(xrow), "+v"(gb1[0]), "+v"(gb1[1]) : "n"(2 * NG) : "memory");
    asm volatile("" ::"v"(pfd));
    STAMPG(1);
    BAR();
    STAMPG(2);
    {
      constexpr bool RD = true;
      RX(0); RW(0, 4); RW(0, 5); RW(0, 0); RW(0, 1); RW(0, 2); RW(0, 3);
      if constexpr (N8) { RW(0, 6); RW(0, 7); }
    }
    waitS(IC<0>{});
    asm volatile("" : "+s"(epoch) :: "memory");
    tag = epoch + 1u;
    if (threadIdx.x == 0) *(volatile AS3 u32*)(L + L_OFL) = epoch;
    phase(IC<0>{}, IC<1>{}, IC<0>{}, nullptr, nullptr, 0u);
    {
      float v[8];
#pragma unroll
      for (int i = 0; i < 4; ++i) { v[2 * i] = bflo(xrow[i]); v[2 * i + 1] = bfhi(xrow[i]); }
      const float mean = wave_sum(((v[0] + v[1]) + (v[2] + v[3])) + ((v[4] + v[5]) + (v[6] + v[7]))) * (1.0f / 512.0f);
      float qq = 0.f;
#pragma unroll
      for (int i = 0; i < 8; ++i) { float d = v[i] - mean; qq = fmaf(d, d, qq); }
      const float rstd = 1.0f / sqrtf(wave_sum(qq) * (1.0f / 512.0f) + 1e-5f);
      if (l == 0) {
        u32x4 gr = {fbits(mean), fbits(rstd), tag, tag};
        st_sc1_x4(ws, WS_STAT + (u32)(128 * p + 8 * c + w) * 16u, gr);
        if (w == 7) st_sc1_x1(ws, WS_AFLAG + (u32)b * 4u, tag);
      }
    }
    auto colsum = [&](int s, u32 sb) {
      if (p < 8 && (p >> 1) == s && w < (TA ? 6 : 7)) {
        u32x2 c0l[2], c0h[2], c1l[2], c1h[2];
        const u32 a0 = (vbase ^ (u32)((2 * (w & 1)) * 32)) + (u32)(w >> 1) * 1024u + sb;
        const u32 a1 = a0 ^ 32u;
        dsrtr<0>(c0l[0], a0); dsrtr<512>(c0h[0], a0); dsrtr<16384>(c0l[1], a0); dsrtr<16384 + 512>(c0h[1], a0);
        dsrtr<0>(c1l[0], a1); dsrtr<512>(c1h[0], a1); dsrtr<16384>(c1l[1], a1); dsrtr<16384 + 512>(c1h[1], a1);
        asm volatile("s_waitcnt lgkmcnt(0)" : "+v"(c0l[0]), "+v"(c0h[0]), "+v"(c0l[1]), "+v"(c0h[1]),
                     "+v"(c1l[0]), "+v"(c1h[0]), "+v"(c1l[1]), "+v"(c1h[1]) :: "memory");
        f32x4 c0 = {0, 0, 0, 0}, c1 = {0, 0, 0, 0};
#pragma unroll
        for (int ks = 0; ks < 2; ++ks) {
          c0 = mfma32(ones, cat(c0l[ks], c0h[ks]), c0);
          c1 = mfma32(ones, cat(c1l[ks], c1h[ks]), c1);
        }
        if (l < 32) {
          u32x2 gr = {fbits(l < 16 ? c0[0] : c1[0]), tag};
          st_sc1_x2(ws, WS_CS + (u32)((c * 256 + 32 * w + l) * 8 + ((s + rot) & 7)) * 8u, gr);
        }
      }
    };
    colsum(0, 0u);
    u32 sb = 0;
#pragma unroll 1
    for (int s = 0; s < 5; ++s) {
      const u32 nsb = sb == 2 * SLOT ? 0u : sb + SLOT;
      advance(nsb - sb);
      const int ks = (s + 3 + rot) & 7;
      WAIT_V(NG);
      STAMPG(3 + s);
      phase(IC<1>{}, IC<1>{}, IC<1>{}, xsrc + ks * 128, wsrc + (size_t)((u32)ks * 64u * wstride), sb);
      phase(IC<0>{}, IC<1>{}, IC<2>{}, xsrc + ks * 128, wsrc + (size_t)((u32)ks * 64u * wstride), sb);
      colsum(s + 1, nsb);
      sb = nsb;
    }
    advance(0u - 2u * SLOT);
    WAIT_V(NG);
    STAMPG(8);
    ld_sc1_x4(gat[0], ws, gcs);
    ld_sc1_x4(gat[1], ws, gcs + 1024u);
    ld_sc1_x4(gat[2], ws, gst);
    if (PF_MODE == 1) prefetchW();
    phase(IC<1>{}, IC<1>{}, IC<0>{}, nullptr, nullptr, 0u);
    if (TA) issueB(0);
    phase(IC<0>{}, IC<1>{}, IC<0>{}, nullptr, nullptr, 0u);
    advance(SLOT);
    WAIT_V((TA ? 7 : 3) + PFN);
    STAMPG(9);
    phase(IC<1>{}, IC<1>{}, IC<0>{}, nullptr, nullptr, 0u);
    if (TA) issueB(1);
    phase(IC<0>{}, IC<1>{}, IC<0>{}, nullptr, nullptr, 0u);
    asm volatile("s_waitcnt vmcnt(%3)" : "+v"(gat[0]), "+v"(gat[1]), "+v"(gat[2]) : "n"((TA ? 8 : 0) + PFN) : "memory");
    {
      const bool used = w < (TA ? 6 : 7);
#pragma unroll 1
      for (int it = 0; it < SPIN_MAX; ++it) {
        const bool bad = (used && ((gat[0][1] != tag) | (gat[0][3] != tag) | (gat[1][1] != tag) | (gat[1][3] != tag))) | (gat[2][2] != tag);
        if (__builtin_amdgcn_ballot_w64(bad) == 0ull) break;
        ld_sc1_x4(gat[0], ws, gcs);
        ld_sc1_x4(gat[1], ws, gcs + 1024u);
        ld_sc1_x4(gat[2], ws, gst);
        asm volatile("s_waitcnt vmcnt(0)" : "+v"(gat[0]), "+v"(gat[1]), "+v"(gat[2]) :: "memory");
      }
#pragma unroll
      for (int i = 0; i < 2; ++i) {
        float sm = bitsf(gat[i][0]) + bitsf(gat[i][2]);
        sm += bitsf((u32)__builtin_amdgcn_update_dpp(0, (int)fbits(sm), 0xB1, 0xf, 0xf, false));
        sm += bitsf((u32)__builtin_amdgcn_update_dpp(0, (int)fbits(sm), 0x4E, 0xf, 0xf, false));
        if ((l & 3) == 0) {
          f32x2 o = {sm, bflo(gb1[i])};
          *(AS3 f32x2*)(L + L_CSB + (u32)(32 * w + 16 * i + (l >> 2)) * 8u) = o;
        }
      }
      if (l < 16) {
        *(AS3 float*)(L + L_MEAN + (u32)(16 * w + l) * 4u) = bitsf(gat[2][0]);
        *(AS3 float*)(L + L_RSTD + (u32)(16 * w + l) * 4u) = bitsf(gat[2][1]);
      }
    }
    waitS(IC<1>{});
    BAR();
    SCHED();
    if constexpr (N8) { MF(1, 6, 0); MF(1, 6, 1); MF(1, 7, 0); MF(1, 7, 1); SCHED(); }
    if constexpr (TA) {
      MFS(1, 4, 0); issueBk(2, 0); SCHED(); MFS(1, 4, 1); issueBk(2, 1); SCHED();
      MFS(1, 5, 0); issueBk(2, 2); SCHED(); MFS(1, 5, 1); issueBk(2, 3); SCHED();
    } else {
      MF(1, 4, 0); MF(1, 4, 1); MF(1, 5, 0); MF(1, 5, 1); SCHED();
    }
    MF(1, 2, 0); if (TA) issueBk(3, 0); SCHED(); MF(1, 2, 1); if (TA) issueBk(3, 1); SCHED();
    MF(1, 3, 0); if (TA) issueBk(3, 2); SCHED(); MF(1, 3, 1); if (TA) issueBk(3, 3); SCHED();
    MF(1, 0, 0); MF(1, 0, 1); MF(1, 1, 0); MF(1, 1, 1); SCHED();
#undef MF
#undef MFS
#undef RW
#undef RX
    STAMP(2);
    STAMPG(10);
  };
  if (typeA) g1(IC<1>{}, IC<0>{});
  else if (cg == 0) g1(IC<0>{}, IC<1>{});
  else g1(IC<0>{}, IC<0>{});
  STAMP(3);

  const float mean0 = *(AS3 float*)(L + L_MEAN + (u32)(32 * rg + r16) * 4u);
  const float mean1 = *(AS3 float*)(L + L_MEAN + (u32)(32 * rg + 16 + r16) * 4u);
  const float rstd0 = *(AS3 float*)(L + L_RSTD + (u32)(32 * rg + r16) * 4u);
  const float rstd1 = *(AS3 float*)(L + L_RSTD + (u32)(32 * rg + 16 + r16) * 4u);

  const u32 vb2 = (u32)(4 * gq + q4) * 128u + (u32)pp * 8u + (u32)((2 * gq + (q4 >> 1)) & 3) * 32u;
  u32x2 blo[2][4], bhi[2][4];
  u32 VB[4];
#pragma unroll
  for (int ct = 0; ct < 4; ++ct) VB[ct] = lb + (vb2 ^ (u32)(ct * 32)) + (u32)w * 4096u;
  auto rdB = [&](auto jc) {
    constexpr int j = decltype(jc)::value, blk = (j + 3) & 3;
#pragma unroll
    for (int ct = 0; ct < 4; ++ct) {
      const u32 a = VB[ct] + (blk >= 2 ? 65536u : 0u);
      dsrtr<(blk & 1) * 32768>(blo[j & 1][ct], a);
      dsrtr<(blk & 1) * 32768 + 2048>(bhi[j & 1][ct], a);
    }
  };
  const int dp = rb >> 1;

  const char* af = ws + WS_AF + (size_t)(4 * rb) * 73728u;
  const char* afh = af + (size_t)(7 * w) * 1024u;
  const char* afo = af + (size_t)(56 + 2 * w) * 1024u;
  const u32 vo = (u32)l * 16u;
  u32x4 a[9][4];
  auto loadA = [&](int j) {
    const char* s = j < 7 ? afh + j * 1024 : afo + (j - 7) * 1024;
#pragma unroll
    for (int rt = 0; rt < 4; ++rt) ld_sc1_x4(a[j][rt], s + rt * 73728, vo);
  };
  auto waitA01 = [&]() {
    asm volatile("s_waitcnt vmcnt(0)" : "+v"(a[0][0]), "+v"(a[0][1]), "+v"(a[0][2]), "+v"(a[0][3]), "+v"(a[1][0]), "+v"(a[1][1]), "+v"(a[1][2]), "+v"(a[1][3])
                 :: "memory");
  };
  u32x4 phq[2][2], zg;
  if (!typeA) {
    auto hpair = [&](auto Pc) {
      constexpr int P = decltype(Pc)::value;
      const int hl = cg * 4 + P;
      u32x4 o0, o1;
#pragma unroll
      for (int e = 0; e < 2; ++e) {
        const int t = 2 * P + e;
        const u32 ca = L_CSB + (u32)(hl * 32 + e * 16 + 4 * gq) * 8u;
        const f32x4 cb0 = lds128f(L, ca), cb1 = lds128f(L, ca + 16u);
        const float cs[4] = {cb0[0], cb0[2], cb1[0], cb1[2]}, bb[4] = {cb0[1], cb0[3], cb1[1], cb1[3]};
        float h0[4], h1[4];
#pragma unroll
        for (int r = 0; r < 4; ++r) {
          float y0 = fmaf(rstd0, fmaf(-mean0, cs[r], acc[t][0][r]), bb[r]);
          float y1 = fmaf(rstd1, fmaf(-mean1, cs[r], acc[t][1][r]), bb[r]);
          y0 = fmaxf(y0, 0.f); y1 = fmaxf(y1, 0.f);
          h0[r] = y0 * y0; h1[r] = y1 * y1;
        }
        o0[2 * e] = pk2(h0[0], h0[1]); o0[2 * e + 1] = pk2(h0[2], h0[3]);
        o1[2 * e] = pk2(h1[0], h1[1]); o1[2 * e + 1] = pk2(h1[2], h1[3]);
      }
      const u32 step = (u32)(7 * g + ((g & 1) ? (hl < 6 ? hl + 1 : 0) : hl));
      const u32 off = WS_AF + ((u32)(8 * p + 2 * rg) * 72u + step) * 1024u + (u32)l * 16u;
      st_sc1_x4(ws, off, o0);
      st_sc1_x4(ws, off + 72u * 1024u, o1);
    };
    hpair(IC<0>{}); hpair(IC<1>{}); hpair(IC<2>{});
    if (cg == 0) hpair(IC<3>{});
    WAIT_V(0);
    BAR();
    if (threadIdx.x == 0) st_sc1_x1(ws, WS_HFLAG + (u32)p * 128u + (u32)g * 4u, tag);
    STAMP(4);
    if (PF_MODE == 2 || PF_MODE == 4) prefetchW();
    issueB(0); issueB(1); issueB(2); issueB(3);
    if (PF_MODE == 3) prefetchW();
    if (w == 0) poll_flags(ws, WS_HFLAG + (u32)dp * 128u, 8, l, tag);
    WAIT_V(0);
    BAR();
    rdB(IC<0>{}); rdB(IC<1>{});
    waitb(blo[0], bhi[0]); waitb(blo[1], bhi[1]);
    issueB(4); issueB(5);
    STAMP(5);
    loadA(0); loadA(1);
    waitA01();
    STAMP(7);
  } else {
    const u32x2 ident = {(4 * gq + 0 == r16 ? 0x3F80u : 0u) | (4 * gq + 1 == r16 ? 0x3F800000u : 0u),
                         (4 * gq + 2 == r16 ? 0x3F80u : 0u) | (4 * gq + 3 == r16 ? 0x3F800000u : 0u)};
    const f32x4 zero4 = {0, 0, 0, 0};
    const f32x4 vm0 = lds128f(L, L_MEAN + (u32)(32 * rg + 4 * gq) * 4u), vm1 = lds128f(L, L_MEAN + (u32)(32 * rg + 16 + 4 * gq) * 4u);
    const f32x4 vr0 = lds128f(L, L_RSTD + (u32)(32 * rg + 4 * gq) * 4u), vr1 = lds128f(L, L_RSTD + (u32)(32 * rg + 16 + 4 * gq) * 4u);
    u32x2 wp[2];
    auto logits = [&](int line, int pi, int rt) -> f32x4 {
      const int t = line * 2 + pi;
      const u32 ca = L_CSB + (u32)(line * 64 + (2 * cg + pi) * 16 + 4 * gq) * 8u;
      const f32x4 cb0 = lds128f(L, ca), cb1 = lds128f(L, ca + 16u);
      const float mn = rt ? mean1 : mean0, rs = rt ? rstd1 : rstd0;
      const float y0 = rs * fmaf(-mn, cb0[0], acc[t][rt][0]), y1 = rs * fmaf(-mn, cb0[2], acc[t][rt][1]);
      const float y2 = rs * fmaf(-mn, cb1[0], acc[t][rt][2]), y3 = rs * fmaf(-mn, cb1[2], acc[t][rt][3]);
      const u32x2 kb = {pk2(y0, y1), pk2(y2, y3)};
      return mfma16(wp[pi], kb, zero4);
    };
#pragma unroll
    for (int pi = 0; pi < 2; ++pi)
      wp[pi] = lds_tr(L, L_WPHI + (u32)((2 * cg + pi) * 512 + (4 * gq + q4) * 32 + pp * 8));
    f32x4 kvt[4], zt[4];
#pragma unroll
    for (int pi = 0; pi < 2; ++pi) {
      u32x4 tp, tn, vv;
      if (pi == 1) { WAIT_V(8); rdB(IC<0>{}); rdB(IC<1>{}); }
#pragma unroll
      for (int rt = 0; rt < 2; ++rt) {
        const u32x4 ph = dual_softmax(logits(1, pi, rt));
        if (pi == 1 && rt == 0) {
          waitb(blo[0], bhi[0]); waitb(blo[1], bhi[1]);
          if (rg == 3) { issueB(4); issueB(5); }
        }
        const f32x4 a = mfma16(u32x2{ph[0], ph[1]}, ident, zero4);
        const f32x4 n = mfma16(u32x2{ph[2], ph[3]}, ident, zero4);
        tp[2 * rt] = pk2(a[0], a[1]); tp[2 * rt + 1] = pk2(a[2], a[3]);
        tn[2 * rt] = pk2(n[0], n[1]); tn[2 * rt + 1] = pk2(n[2], n[3]);
        const float cs = *(AS3 float*)(L + L_CSB + (u32)(128 + (2 * cg + pi) * 16 + r16) * 8u);
        const f32x4 vm = rt ? vm1 : vm0, vr = rt ? vr1 : vr0;
        const f32x4 av = acc[4 + pi][rt];
        vv[2 * rt] = pk2(vr[0] * fmaf(-vm[0], cs, av[0]), vr[1] * fmaf(-vm[1], cs, av[1]));
        vv[2 * rt + 1] = pk2(vr[2] * fmaf(-vm[2], cs, av[2]), vr[3] * fmaf(-vm[3], cs, av[3]));
      }
      kvt[2 * pi] = mfma32(tp, vv, zero4); kvt[2 * pi + 1] = mfma32(tn, vv, zero4);
      zt[2 * pi] = mfma32(tp, ones, zero4); zt[2 * pi + 1] = mfma32(tn, ones, zero4);
    }
    {
      const int sl = rg > 0 ? rg - 1 : 0;
#pragma unroll
      for (int pi = 0; pi < 2; ++pi) {
        if (rg != pi) {
#pragma unroll
          for (int sg = 0; sg < 2; ++sg)
            *(AS3 f32x4*)(L + L_SCR + (u32)((((cg * 2 + pi) * 2 + sg) * 3 + sl) * 1024 + l * 16)) = kvt[2 * pi + sg];
        }
        if (r16 == 0) {
#pragma unroll
          for (int sg = 0; sg < 2; ++sg)
            *(AS3 f32x4*)(L + L_ZR + (u32)(((((cg * 2 + pi) * 2 + sg) * 4 + rg) * 16 + 4 * gq) * 4)) = zt[2 * pi + sg];
        }
      }
    }
    WAIT_L0();
    BAR();
    if (rg < 2) {
      f32x4 sp = rg == 0 ? kvt[0] : kvt[2], sn = rg == 0 ? kvt[1] : kvt[3];
#pragma unroll
      for (int sl = 0; sl < 3; ++sl) {
        sp += lds128f(L, L_SCR + (u32)((((cg * 2 + rg) * 2 + 0) * 3 + sl) * 1024 + l * 16));
        sn += lds128f(L, L_SCR + (u32)((((cg * 2 + rg) * 2 + 1) * 3 + sl) * 1024 + l * 16));
      }
      u32x4 o = {pk2(sp[0], sp[1]), pk2(sp[2], sp[3]), pk2(sn[0], sn[1]), pk2(sn[2], sn[3])};
      st_sc1_x4(ws, WS_KVP + (u32)(((g * 16 + p) * 4 + 2 * cg + rg) * 1024 + l * 16), o);
    } else if (rg == 2 && l < 16) {
      const int pi = l >> 3, sg = (l >> 2) & 1, qq = l & 3;
      f32x4 s = {0, 0, 0, 0};
#pragma unroll
      for (int r = 0; r < 4; ++r) s += lds128f(L, L_ZR + (u32)(((((cg * 2 + pi) * 2 + sg) * 4 + r) * 16 + 4 * qq) * 4));
      st_sc1_x4(ws, WS_ZP + (u32)((((g * 4 + 2 * cg + pi) * 16 + p) * 32 + (l & 7) * 4) * 4), __builtin_bit_cast(u32x4, s));
    }
#pragma unroll
    for (int rt = 0; rt < 2; ++rt) phq[0][rt] = dual_softmax(logits(0, 0, rt));
    if (rg != 3) WAIT_V(0);
    BAR();
    if (threadIdx.x == 0) st_sc1_x1(ws, WS_KVFLAG + (u32)g * 128u + (u32)p * 4u, tag);
    STAMP(4);
    if (rg != 3) { issueB(4); issueB(5); }
#pragma unroll
    for (int rt = 0; rt < 2; ++rt) phq[1][rt] = dual_softmax(logits(0, 1, rt));
    if (w == 3)
      poll_o(ws, (l & 31) < 16 ? WS_KVFLAG + (u32)g * 128u + (u32)(l & 15) * 4u : WS_HFLAG + (u32)dp * 128u + (u32)(l & 7) * 4u, tag);
    BAR();
    STAMP(5);
    {
      const int hd = w & 3, hf = w >> 2;
#pragma unroll
      for (int i = 0; i < 8; ++i) ld_sc1_x4(gat[i], ws, WS_KVP + (u32)(((g * 16 + 8 * hf + i) * 4 + hd) * 1024 + l * 16));
      ld_sc1_x4(zg, ws, WS_ZP + (u32)(((g * 4 + hd) * 16 + 8 * hf) * 128 + l * 16));
      BAR();
      loadA(0); loadA(1);
      asm volatile("s_waitcnt vmcnt(8)" : "+v"(gat[0]), "+v"(gat[1]), "+v"(gat[2]), "+v"(gat[3]), "+v"(gat[4]), "+v"(gat[5]),
                   "+v"(gat[6]), "+v"(gat[7]), "+v"(zg) :: "memory");
    }
    STAMP(6);
    {
      const int hd = w & 3, hf = w >> 2;
      f32x4 s0 = {0, 0, 0, 0}, s1 = {0, 0, 0, 0};
#pragma unroll
      for (int i = 0; i < 8; ++i) {
        s0[0] += bflo(gat[i][0]); s0[1] += bfhi(gat[i][0]); s0[2] += bflo(gat[i][1]); s0[3] += bfhi(gat[i][1]);
        s1[0] += bflo(gat[i][2]); s1[1] += bfhi(gat[i][2]); s1[2] += bflo(gat[i][3]); s1[3] += bfhi(gat[i][3]);
      }
      *(AS3 f32x4*)(L + L_SCR + (u32)((hd * 2 + hf) * 2048 + l * 32)) = s0;
      *(AS3 f32x4*)(L + L_SCR + (u32)((hd * 2 + hf) * 2048 + l * 32 + 16)) = s1;
      f32x4 zs = __builtin_bit_cast(f32x4, zg);
#pragma unroll
      for (int r = 0; r < 4; ++r) {
        float v = zs[r];
        v += __shfl_xor(v, 8); v += __shfl_xor(v, 16); v += __shfl_xor(v, 32);
        zs[r] = v;
      }
      if (l < 8) *(AS3 f32x4*)(L + L_ZR + (u32)(((hd * 2 + hf) * 32 + l * 4) * 4)) = zs;
    }
    WAIT_L0();
    BAR();
    {
      u32x4 o0, o1;
#pragma unroll
      for (int pi = 0; pi < 2; ++pi) {
        const int hd = 2 * cg + pi;
        const u32 ga = L_SCR + (u32)(hd * 4096 + l * 32);
        const f32x4 kp = lds128f(L, ga) + lds128f(L, ga + 2048u), kn = lds128f(L, ga + 16u) + lds128f(L, ga + 2064u);
        const u32 za = L_ZR + (u32)((hd * 64 + 4 * gq) * 4);
        const f32x4 zp = lds128f(L, za) + lds128f(L, za + 128u), zn = lds128f(L, za + 64u) + lds128f(L, za + 192u);
        const u32x4 kf = {pk2(kp[0], kp[1]), pk2(kp[2], kp[3]), pk2(kn[0], kn[1]), pk2(kn[2], kn[3])};
        const u32x4 zf = {pk2(zp[0], zp[1]), pk2(zp[2], zp[3]), pk2(zn[0], zn[1]), pk2(zn[2], zn[3])};
#pragma unroll
        for (int rt = 0; rt < 2; ++rt) {
          const f32x4 num = mfma32(kf, phq[pi][rt], zero4), den = mfma32(zf, phq[pi][rt], zero4);
          const float rd = __builtin_amdgcn_rcpf(den[0] + 1e-6f);
          const u32 a = pk2(num[0] * rd, num[1] * rd), bq = pk2(num[2] * rd, num[3] * rd);
          if (rt == 0) { o0[2 * pi] = a; o0[2 * pi + 1] = bq; } else { o1[2 * pi] = a; o1[2 * pi + 1] = bq; }
        }
      }
      const u32 off = WS_AF + ((u32)(8 * p + 2 * rg) * 72u + (u32)(56 + 2 * g + cg)) * 1024u + (u32)l * 16u;
      waitA01();
      st_sc1_x4(ws, off, o0);
      st_sc1_x4(ws, off + 72u * 1024u, o1);
    }
    BAR();
    STAMP(7);
  }
  {
    u32x4 xres, b2v;
    const u32 fo = (u32)(64 * rb + (threadIdx.x >> 3)) * 1024u + (u32)(cb * 128 + (threadIdx.x & 7) * 16);
    f32x4 dacc[4][4];
#pragma unroll
    for (int i = 0; i < 4; ++i)
#pragma unroll
      for (int j = 0; j < 4; ++j) dacc[i][j] = f32x4{0, 0, 0, 0};
    auto waitA = [&](int j, auto n) { wait4<decltype(n)::value>(a[j][0], a[j][1], a[j][2], a[j][3]); };
    auto step = [&](int j) {
#pragma unroll
      for (int ct = 0; ct < 4; ++ct) {
        const u32x4 bf = cat(blo[j & 1][ct], bhi[j & 1][ct]);
#pragma unroll
        for (int rt = 0; rt < 4; ++rt) dacc[rt][ct] = mfma32(bf, a[j][rt], dacc[rt][ct]);
      }
      SCHED();
    };
    loadA(2); loadA(3);
    step(0); rdB(IC<2>{});
    step(1); waitb(blo[0], bhi[0]); rdB(IC<3>{});
    WAIT_V(8);
    if (typeA && l == 0) st_sc1_x1(ws, WS_OFLAG + (u32)((p * 8 + g) * 8 + w) * 4u, tag);
    STAMP(8);
    issueB(6); loadA(4);
    waitA(2, IC<12>{}); step(2); waitb(blo[1], bhi[1]); rdB(IC<4>{}); issueB(7); loadA(5);
    waitA(3, IC<16>{}); step(3); waitb(blo[0], bhi[0]); rdB(IC<5>{}); issueB(8); loadA(6);
    waitA(4, IC<16>{}); step(4); waitb(blo[1], bhi[1]); rdB(IC<6>{});
    waitA(5, IC<8>{}); step(5); waitb(blo[0], bhi[0]); rdB(IC<7>{});
    waitA(6, IC<0>{}); step(6); waitb(blo[1], bhi[1]); rdB(IC<8>{});
    STAMP(9);
    if (w == 0) {
      poll_o(ws, WS_OFLAG + (u32)((dp * 8 + ((l >> 2) & 7)) * 8 + 2 * (rb & 1) + (l & 1) + 4 * ((l >> 1) & 1)) * 4u, tag);
      *(volatile AS3 u32*)(L + L_OFL) = tag;
    } else {
#pragma unroll 1
      for (int it = 0; it < SPIN_MAX * 8; ++it) {
        if ((u32)__builtin_amdgcn_readfirstlane((int)*(volatile AS3 u32*)(L + L_OFL)) == tag) break;
        __builtin_amdgcn_s_sleep(2);
      }
    }
    STAMP(10);
    loadA(7); loadA(8);
    ld_x4(xres, x, fo);
    ld_x4(b2v, b2, (u32)(cb * 128 + (threadIdx.x & 7) * 16));
    STAMP(13);
    waitA(7, IC<6>{}); step(7);
    waitA(8, IC<2>{}); waitb(blo[0], bhi[0]); step(8);
    STAMP(14);
    WAIT_L0();
#pragma unroll
    for (int rt = 0; rt < 4; ++rt)
#pragma unroll
      for (int ct = 0; ct < 4; ++ct)
        *(AS3 f32x4*)(L + dslot(rt) + (u32)(r16 * 256 + (((ct * 4 + gq) ^ r16) * 16))) = dacc[rt][ct];
    WAIT_L0();
    BAR();
    STAMP(11);
    {
      const int row = threadIdx.x >> 3, cq = threadIdx.x & 7, rt = row >> 4, tk = row & 15;
      const u32 pb = (u32)((rt + 3) & 3) * 32768u + (u32)(tk * 256);
      f32x4 s0 = {0, 0, 0, 0}, s1 = {0, 0, 0, 0};
#pragma unroll
      for (int ww = 0; ww < 8; ++ww) {
        s0 += lds128f(L, pb + (u32)(ww * 4096) + (u32)(((2 * cq) ^ tk) * 16));
        s1 += lds128f(L, pb + (u32)(ww * 4096) + (u32)(((2 * cq + 1) ^ tk) * 16));
      }
      asm volatile("s_waitcnt vmcnt(0)" : "+v"(xres), "+v"(b2v) :: "memory");
      u32x4 o;
      o[0] = pk2(s0[0] + bflo(xres[0]) + bflo(b2v[0]), s0[1] + bfhi(xres[0]) + bfhi(b2v[0]));
      o[1] = pk2(s0[2] + bflo(xres[1]) + bflo(b2v[1]), s0[3] + bfhi(xres[1]) + bfhi(b2v[1]));
      o[2] = pk2(s1[0] + bflo(xres[2]) + bflo(b2v[2]), s1[1] + bfhi(xres[2]) + bfhi(b2v[2]));
      o[3] = pk2(s1[2] + bflo(xres[3]) + bflo(b2v[3]), s1[3] + bfhi(xres[3]) + bfhi(b2v[3]));
      *(u32x4*)(out + fo) = o;
    }
    if (b == 255 && w == 7) {
#pragma unroll 1
      for (int i = 0; i < 4; ++i) poll_flags(ws, WS_AFLAG + (u32)i * 256u, 64, l, tag);
      if (l == 0) st_sc1_x1(ws, WS_EPOCH, tag);
    }
    STAMP(12);
    STAMP_FLUSH;
  }
}

extern "C" void kernel_launch(void* const* d_in, const int* in_sizes, int n_in, void* d_out, int out_size, void* d_ws,
                              size_t ws_size, hipStream_t stream) {
  (void)in_sizes; (void)n_in; (void)out_size; (void)ws_size;
  hipFuncSetAttribute((const void*)fused_kernel, hipFuncAttributeMaxDynamicSharedMemorySize, LDS_BYTES);
  fused_kernel<<<256, 512, LDS_BYTES, stream>>>((const char*)d_in[0], (const char*)d_in[1], (const char*)d_in[2],
                                                (const char*)d_in[3], (const char*)d_in[5], (const char*)d_in[6],
                                                (const char*)d_in[7], (const char*)d_in[8], (char*)d_out, (char*)d_ws);
}
```

```cpp
#include <hip/hip_runtime.h>
#include <stdint.h>

#define EXP_NODMA 0
#define EXP_NOBAR 0
typedef unsigned int u32;
typedef unsigned long long u64;
typedef __bf16 bf16x8 __attribute__((ext_vector_type(8)));
typedef __bf16 bf16x2 __attribute__((ext_vector_type(2)));
typedef short v4i16 __attribute__((ext_vector_type(4)));
typedef float f32x4 __attribute__((ext_vector_type(4)));
typedef float f32x2 __attribute__((ext_vector_type(2)));
typedef u32 u32x4 __attribute__((ext_vector_type(4)));
typedef u32 u32x2 __attribute__((ext_vector_type(2)));

#define AS1 __attribute__((address_space(1)))
#define AS3 __attribute__((address_space(3)))
#define AS4 __attribute__((address_space(4)))
#define DEVI __device__ __forceinline__

constexpr u32 WS_EPOCH = 0;
constexpr u32 WS_AFLAG = 1024;
constexpr u32 WS_HFLAG = 2048;
constexpr u32 WS_OFLAG = 4096;
constexpr u32 WS_STAT = 8192;
constexpr u32 WS_CS = 40960;
constexpr u32 WS_KVFLAG = 0x4A000;
constexpr u32 WS_KVP = 0x50000;
constexpr u32 WS_ZP = 0xD0000;
constexpr u32 WS_AF = 0x100000;
constexpr u32 WS_TIM = 0xB00000;
constexpr u32 WS_USED = 0xC00000;

constexpr u32 SLOT = 49152;
constexpr u32 L_SCR = 131072;
constexpr u32 L_ZR = L_SCR + 24576;
constexpr u32 L_CSB = 157696;
constexpr u32 L_MEAN = 159744;
constexpr u32 L_RSTD = 160256;
constexpr u32 L_WPHI = 160768;
constexpr u32 L_PF = 162816;
constexpr u32 L_OFL = 163072;
constexpr u32 LDS_BYTES = 163328;
constexpr u32 CODE_LINES = 296;

DEVI f32x4 mfma32(u32x4 a, u32x4 b, f32x4 c) {
  return __builtin_amdgcn_mfma_f32_16x16x32_bf16(__builtin_bit_cast(bf16x8, a), __builtin_bit_cast(bf16x8, b), c, 0, 0, 0);
}
DEVI f32x4 mfma16(u32x2 a, u32x2 b, f32x4 c) {
  return __builtin_amdgcn_mfma_f32_16x16x16bf16_1k(__builtin_bit_cast(v4i16, a), __builtin_bit_cast(v4i16, b), c, 0, 0, 0);
}
DEVI u32 pk2(float a, float b) {
  f32x2 v = {a, b};
  return __builtin_bit_cast(u32, __builtin_convertvector(v, bf16x2));
}
DEVI float bflo(u32 v) { return __builtin_bit_cast(float, v << 16); }
DEVI float bfhi(u32 v) { return __builtin_bit_cast(float, v & 0xffff0000u); }
DEVI u32 fbits(float f) { return __builtin_bit_cast(u32, f); }
DEVI float bitsf(u32 u) { return __builtin_bit_cast(float, u); }

DEVI u32x2 lds_tr(AS3 char* L, u32 addr) {
  v4i16 t = __builtin_amdgcn_ds_read_tr16_b64_v4i16((AS3 v4i16*)(L + addr));
  return __builtin_bit_cast(u32x2, t);
}
DEVI u32x4 lds_frag_tr(AS3 char* L, u32 addr, u32 second) {
  u32x2 a = lds_tr(L, addr), b = lds_tr(L, addr + second);
  u32x4 r = {a[0], a[1], b[0], b[1]};
  return r;
}
DEVI u32x4 lds128(AS3 char* L, u32 addr) { return *(AS3 u32x4*)(L + addr); }
DEVI f32x4 lds128f(AS3 char* L, u32 addr) { return *(AS3 f32x4*)(L + addr); }
template <int OFF> DEVI void glds_m0(u32 m0v, const char* sbase, u32 voff) {
  asm volatile("s_mov_b32 m0, %0\n\ts_nop 0\n\tglobal_load_lds_dwordx4 %1, %2 offset:%3" ::"s"(m0v), "v"(voff), "s"(sbase), "n"(OFF) : "memory");
}
template <int OFF> DEVI void glds(const char* sbase, u32 voff) {
  asm volatile("global_load_lds_dwordx4 %0, %1 offset:%2" ::"v"(voff), "s"(sbase), "n"(OFF) : "memory");
}
template <int OFF> DEVI void glds_masked(const char* sbase, u32 voff, u64 mask) {
  asm volatile("s_mov_b64 exec, %3\n\tglobal_load_lds_dwordx4 %0, %1 offset:%2\n\ts_mov_b64 exec, -1" ::"v"(voff), "s"(sbase), "n"(OFF), "s"(mask) : "memory");
}
DEVI void pf_lds(u32 m0v, const char* sbase, u32 voff, u64 mask) {
  asm volatile("s_mov_b32 m0, %0\n\ts_mov_b64 exec, %3\n\tglobal_load_lds_dword %1, %2\n\ts_mov_b64 exec, -1" ::"s"(m0v), "v"(voff), "s"(sbase), "s"(mask) : "memory");
}
template <int OFF> DEVI void dsr128(u32x4& d, u32 a) {
  asm volatile("ds_read_b128 %0, %1 offset:%2" : "=v"(d) : "v"(a), "n"(OFF) : "memory");
}
template <int OFF> DEVI void dsrtr(u32x2& d, u32 a) {
  asm volatile("ds_read_b64_tr_b16 %0, %1 offset:%2" : "=v"(d) : "v"(a), "n"(OFF) : "memory");
}
DEVI u32x4 cat(u32x2 a, u32x2 b) { u32x4 r = {a[0], a[1], b[0], b[1]}; return r; }
#define WAIT_V(n) asm volatile("s_waitcnt vmcnt(%0)" ::"n"(n) : "memory")
#define WAIT_L0() asm volatile("s_waitcnt lgkmcnt(0)" ::: "memory")
#define BAR() __builtin_amdgcn_s_barrier()
#define SCHED() __builtin_amdgcn_sched_barrier(0)

DEVI void ld_sc1_x4(u32x4& d, const char* base, u32 off) {
  asm volatile("global_load_dwordx4 %0, %1, %2 sc1" : "=v"(d) : "v"(off), "s"(base) : "memory");
}
DEVI void ld_x4(u32x4& d, const char* base, u32 off) {
  asm volatile("global_load_dwordx4 %0, %1, %2" : "=v"(d) : "v"(off), "s"(base) : "memory");
}
DEVI void ld_x1(u32& d, const char* base, u32 off) {
  asm volatile("global_load_dword %0, %1, %2" : "=v"(d) : "v"(off), "s"(base) : "memory");
}
DEVI void ld_u16(u32& d, const char* base, u32 off) {
  asm volatile("global_load_ushort %0, %1, %2" : "=v"(d) : "v"(off), "s"(base) : "memory");
}
DEVI u32 ld_sc1_x1_wait(const char* base, u32 off) {
  u32 d;
  asm volatile("global_load_dword %0, %1, %2 sc1\n\ts_waitcnt vmcnt(0)" : "=v"(d) : "v"(off), "s"(base) : "memory");
  return d;
}
DEVI void st_sc1_x4(char* base, u32 off, u32x4 v) {
  asm volatile("global_store_dwordx4 %0, %1, %2 sc1\n\ts_nop 1" ::"v"(off), "v"(v), "s"(base) : "memory");
}
DEVI void st_sc1_x2(char* base, u32 off, u32x2 v) {
  asm volatile("global_store_dwordx2 %0, %1, %2 sc1" ::"v"(off), "v"(v), "s"(base) : "memory");
}
DEVI void st_sc1_x1(char* base, u32 off, u32 v) {
  asm volatile("global_store_dword %0, %1, %2 sc1" ::"v"(off), "v"(v), "s"(base) : "memory");
}
constexpr int SPIN_MAX = 1 << 17;
DEVI void poll_flags(const char* base, u32 off0, int n, int l, u32 tag) {
  u32 off = off0 + (u32)(l < n ? l : 0) * 4u;
#pragma unroll 1
  for (int it = 0; it < SPIN_MAX; ++it) {
    u32 v = ld_sc1_x1_wait(base, off);
    if (__builtin_amdgcn_ballot_w64(v != tag) == 0ull) break;
    __builtin_amdgcn_s_sleep(1);
  }
  __builtin_amdgcn_fence(__ATOMIC_ACQUIRE, "wavefront");
}

#define POLL_SLEEP 1
DEVI void poll_o(const char* base, u32 off, u32 tag) {
#pragma unroll 1
  for (int it = 0; it < SPIN_MAX; ++it) {
    u32 v = ld_sc1_x1_wait(base, off);
    if (__builtin_amdgcn_ballot_w64(v != tag) == 0ull) break;
    __builtin_amdgcn_s_sleep(POLL_SLEEP);
  }
  __builtin_amdgcn_fence(__ATOMIC_ACQUIRE, "wavefront");
}

#define STAMP(i) do {} while (0)
#define STAMPG(i) do {} while (0)
#define STAMP_DECL do {} while (0)
#define STAMP_FLUSH do {} while (0)

DEVI void red4_max2(float& a, float& b) {
  auto s = __builtin_amdgcn_permlane32_swap(fbits(a), fbits(b), false, false);
  float t = fmaxf(bitsf(s[0]), bitsf(s[1]));
  auto u = __builtin_amdgcn_permlane16_swap(fbits(t), fbits(t), false, false);
  float m = fmaxf(bitsf(u[0]), bitsf(u[1]));
  auto z = __builtin_amdgcn_permlane32_swap(fbits(m), fbits(m), false, false);
  a = bitsf(z[0]); b = bitsf(z[1]);
}
DEVI void red4_sum2_rcp(float& a, float& b) {
  auto s = __builtin_amdgcn_permlane32_swap(fbits(a), fbits(b), false, false);
  float t = bitsf(s[0]) + bitsf(s[1]);
  auto u = __builtin_amdgcn_permlane16_swap(fbits(t), fbits(t), false, false);
  float m = __builtin_amdgcn_rcpf(bitsf(u[0]) + bitsf(u[1]));
  auto z = __builtin_amdgcn_permlane32_swap(fbits(m), fbits(m), false, false);
  a = bitsf(z[0]); b = bitsf(z[1]);
}
DEVI u32x4 dual_softmax(f32x4 p) {
  const float L2E = 1.4426950408889634f;
  float mx = fmaxf(fmaxf(p[0], p[1]), fmaxf(p[2], p[3]));
  float nm = fmaxf(fmaxf(-p[0], -p[1]), fmaxf(-p[2], -p[3]));
  red4_max2(mx, nm);
  float a = -mx * L2E, c = -nm * L2E;
  float e0 = __builtin_amdgcn_exp2f(fmaf(p[0], L2E, a)), e1 = __builtin_amdgcn_exp2f(fmaf(p[1], L2E, a));
  float e2 = __builtin_amdgcn_exp2f(fmaf(p[2], L2E, a)), e3 = __builtin_amdgcn_exp2f(fmaf(p[3], L2E, a));
  float n0 = __builtin_amdgcn_exp2f(fmaf(p[0], -L2E, c)), n1 = __builtin_amdgcn_exp2f(fmaf(p[1], -L2E, c));
  float n2 = __builtin_amdgcn_exp2f(fmaf(p[2], -L2E, c)), n3 = __builtin_amdgcn_exp2f(fmaf(p[3], -L2E, c));
  float sp = (e0 + e1) + (e2 + e3), sn = (n0 + n1) + (n2 + n3);
  red4_sum2_rcp(sp, sn);
  u32x4 r = {pk2(e0 * sp, e1 * sp), pk2(e2 * sp, e3 * sp), pk2(n0 * sn, n1 * sn), pk2(n2 * sn, n3 * sn)};
  return r;
}

#define PF_MODE 4
#define PF_EARLY 0
constexpr int PFN = PF_MODE == 1 ? 2 : PF_EARLY;
template <int V> struct IC { static constexpr int value = V; };
template <int N> DEVI void wait4(u32x4& a, u32x4& b, u32x4& c, u32x4& d) {
  asm volatile("s_waitcnt vmcnt(%4)" : "+v"(a), "+v"(b), "+v"(c), "+v"(d) : "n"(N) : "memory");
}
template <int N> DEVI void wait1(u32x4& a) { asm volatile("s_waitcnt vmcnt(%1)" : "+v"(a) : "n"(N) : "memory"); }

template <int N> DEVI void waitset(u32x4& a, u32x4& b, u32x2 (&lo)[8], u32x2 (&hi)[8]);
template <> DEVI void waitset<8>(u32x4& a, u32x4& b, u32x2 (&lo)[8], u32x2 (&hi)[8]) {
  asm volatile("s_waitcnt lgkmcnt(0)"
               : "+v"(a), "+v"(b), "+v"(lo[0]), "+v"(lo[1]), "+v"(lo[2]), "+v"(lo[3]), "+v"(lo[4]), "+v"(lo[5]), "+v"(lo[6]), "+v"(lo[7]),
                 "+v"(hi[0]), "+v"(hi[1]), "+v"(hi[2]), "+v"(hi[3]), "+v"(hi[4]), "+v"(hi[5]), "+v"(hi[6]), "+v"(hi[7])
               :: "memory");
}
template <> DEVI void waitset<6>(u32x4& a, u32x4& b, u32x2 (&lo)[8], u32x2 (&hi)[8]) {
  asm volatile("s_waitcnt lgkmcnt(0)"
               : "+v"(a), "+v"(b), "+v"(lo[0]), "+v"(lo[1]), "+v"(lo[2]), "+v"(lo[3]), "+v"(lo[4]), "+v"(lo[5]),
                 "+v"(hi[0]), "+v"(hi[1]), "+v"(hi[2]), "+v"(hi[3]), "+v"(hi[4]), "+v"(hi[5])
               :: "memory");
}
DEVI void waitb(u32x2 (&lo)[4], u32x2 (&hi)[4]) {
  asm volatile("s_waitcnt lgkmcnt(0)"
               : "+v"(lo[0]), "+v"(lo[1]), "+v"(lo[2]), "+v"(lo[3]), "+v"(hi[0]), "+v"(hi[1]), "+v"(hi[2]), "+v"(hi[3]) :: "memory");
}
DEVI float wave_sum(float v) {
#define DPP_ADD(ctrl, rmask) v += bitsf((u32)__builtin_amdgcn_update_dpp(0, (int)fbits(v), ctrl, rmask, 0xf, false))
  DPP_ADD(0x111, 0xf); DPP_ADD(0x112, 0xf); DPP_ADD(0x114, 0xf); DPP_ADD(0x118, 0xf);
  DPP_ADD(0x142, 0xa); DPP_ADD(0x143, 0xc);
#undef DPP_ADD
  return bitsf((u32)__builtin_amdgcn_readlane((int)fbits(v), 63));
}

extern "C" __global__ void __launch_bounds__(512, 2)
fused_kernel(const char* __restrict__ x, const char* __restrict__ wqkv, const char* __restrict__ wphi,
             const char* __restrict__ wo, const char* __restrict__ w1, const char* __restrict__ b1,
             const char* __restrict__ w2, const char* __restrict__ b2, char* __restrict__ out, char* __restrict__ ws) {
  extern __shared__ __attribute__((aligned(16))) char smem[];
  AS3 char* L = (AS3 char*)smem;
  const int b = blockIdx.x;
  const int l = threadIdx.x & 63;
  const int w = __builtin_amdgcn_readfirstlane(threadIdx.x >> 6);
  u32 pfd;
  {
    u64 pc;
    asm volatile("s_getpc_b64 %0" : "=s"(pc));
    const u32 ln = (u32)(w * 40 + (l < 40 ? l : 39));
    ld_x1(pfd, (const char*)(pc & ~127ull), (ln < CODE_LINES ? ln : CODE_LINES - 1u) * 128u);
  }
  asm volatile("" ::"s"(x), "s"(wqkv), "s"(wphi), "s"(wo), "s"(w1), "s"(b1), "s"(w2), "s"(b2), "s"(out), "s"(ws));
  const int xcd = b & 7, bi = b >> 3;
  const int p = (xcd >> 2) * 8 + (bi >> 2), c = (xcd & 3) * 4 + (bi & 3), g = c >> 1;
  const bool typeA = (c & 1) == 0;
  const int rg = w & 3, cg = w >> 2;
  const int gq = l >> 4, r16 = l & 15, q4 = (l >> 2) & 3, pp = l & 3;
  const int rot = (p & 1) * 4;
  const int rb = 4 * xcd + (bi >> 3), cb = bi & 7;
  STAMP_DECL;
  STAMP(0);

  const u32 lb = (u32)(size_t)L;
  const u32 xrow_off = (u32)(128 * p + 8 * c + w) * 1024u + (u32)l * 16u;
  u32 gb1[2];
  const u32 gb1_off = (u32)(224 * g + (w < 6 ? 32 * w + ((g & 1) ? 32 : 0) : (w == 6 && !(g & 1) ? 192 : 0)) + (l >> 2)) * 2u;
  u32 tag;

  const char* xsrc = x + (size_t)p * 131072u;
  const u32 wstride = typeA ? 3072u : 3584u;
  const char* wsrc = (typeA ? (wqkv + g * 128) : (w1 + 448 * g)) - 4096;
  const u32 vx0 = (u32)(16 * w + (l >> 3)) * 1024u + (u32)(((l & 7) ^ (l >> 4)) * 16);
  const u32 vx1 = (u32)(16 * w + 7 + (l >> 3)) * 1024u + (u32)(((l & 7) ^ (4 + (l >> 4))) * 16);
  const u32 wsp = (u32)(((l & 7) >> 1) ^ (((l >> 4) & 1) | ((w & 1) << 1)));
  const u32 vw = (u32)(8 * w + (l >> 3)) * wstride + (wsp * 2u + (u32)(l & 1)) * 16u + 4096u;
  u32 vws[4];
  {
    const u32 odd = (u32)(g & 1) * 64u;
    vws[0] = typeA ? vw : vw + odd;
    vws[1] = typeA ? vw : vw + odd + 128u - 1024u;
    vws[2] = typeA ? vw : vw + odd + 256u - 2048u;
    vws[3] = vw + (odd ? 0u : 384u) - 3072u;
  }
  const u64 hmask = __builtin_amdgcn_ballot_w64(wsp < 2u);
  const u32 m0x = lb + (u32)w * 2048u, m0w = lb + 16384u + (u32)w * 4096u;

  const char* dsrc2 = w2 + (size_t)(7 * w) * 32768u + cb * 128;
  const char* dsrco = wo + (size_t)(2 * w) * 32768u + cb * 128;
  const u32 vdb = (u32)(l >> 3) * 1024u + (u32)(((((l & 7) >> 1) ^ ((l >> 4) & 3)) * 2 + (l & 1)) * 16);
  auto dslot = [&](int j) -> u32 { return (u32)((j + 3) & 3) * 32768u + (u32)w * 4096u; };
  auto issueB = [&](int j) {
    const char* s = (j < 7) ? (dsrc2 + j * 32768) : (dsrco + (j - 7) * 32768);
    glds_m0<0>((u32)__builtin_amdgcn_readfirstlane((int)(lb + dslot(j))), s, vdb);
    glds<1024>(s, vdb + 7168u);
    glds<2048>(s, vdb + 2u * 7168u);
    glds<3072>(s, vdb + 3u * 7168u);
  };

  auto issueBk = [&](int j, int k) {
    const char* s = (j < 7) ? (dsrc2 + j * 32768) : (dsrco + (j - 7) * 32768);
    if (k == 0) glds_m0<0>(lb + dslot(j), s, vdb);
    if (k == 1) glds<1024>(s, vdb + 7168u);
    if (k == 2) glds<2048>(s, vdb + 2u * 7168u);
    if (k == 3) glds<3072>(s, vdb + 3u * 7168u);
  };

  auto prefetchW = [&]() {
    const u32 wi = (u32)((bi >> 1) * 8 + w);
    const u32 row = 112u * (wi >> 4) + 64u + 3u * (wi & 15u) + (u32)(l < 48 ? l >> 4 : 0);
    pf_lds(lb + L_PF, w2, row * 2048u + (u32)(l & 15) * 128u, 0x0000ffffffffffffull);
    pf_lds(lb + L_PF, wo, (wi * 4u) * 1024u + (u32)(l & 31) * 128u, 0xffffffffull);
  };
  auto prefetchK = [&](int k) {
    const u32 wi = (u32)((bi >> 1) * 2 + cg);
    if (k < 3) pf_lds(lb + L_PF, w2, (wi >> 2) * 229376u + 131072u + ((wi & 3u) * 192u + (u32)(k * 64 + l)) * 128u, ~0ull);
    else pf_lds(lb + L_PF, wo, (wi * 128u + (u32)((k - 3) * 64 + l)) * 128u, ~0ull);
  };
  u32 xa0 = lb + (u32)(32 * rg + r16) * 128u + (u32)((gq ^ ((r16 >> 1) & 7)) * 16);
  u32 xa1 = xa0 ^ 64u;
  const u32 vbase = lb + 16384u + (u32)gq * 4096u + (u32)q4 * 128u + (u32)pp * 8u + (u32)(((q4 >> 1) & 1) | ((gq & 1) << 1)) * 32u;
  u32 R[8];
#pragma unroll
  for (int t = 0; t < 8; ++t) {
    const u32 ra = (vbase ^ (u32)((2 * cg + (t & 1)) * 32)) + (u32)(t >> 1) * 1024u;
    const u32 rbb = (vbase ^ (u32)((t & 3) * 32)) + (u32)(t >> 2) * 1024u + (u32)cg * 2048u;
    R[t] = typeA ? ra : rbb;
  }

  f32x4 acc[8][2];
#pragma unroll
  for (int t = 0; t < 8; ++t) { acc[t][0] = f32x4{0, 0, 0, 0}; acc[t][1] = f32x4{0, 0, 0, 0}; }
  u32x4 gat[8];
  const u32x4 ones = {0x3F803F80u, 0x3F803F80u, 0x3F803F80u, 0x3F803F80u};
  const u32 gcs = WS_CS + (u32)(c * 256 + 32 * w + (l >> 2)) * 64u + (u32)(l & 3) * 16u;
  const u32 gst = WS_STAT + (u32)(128 * p + 16 * w + r16) * 16u;

  auto g1 = [&](auto TAc, auto N8c) {
    constexpr bool TA = decltype(TAc)::value != 0, N8 = decltype(N8c)::value != 0;
    constexpr int NG = TA ? 5 : 6;
    u32x4 fx[2][2];
    u32x2 flo[2][8], fhi[2][8];
    u32x4 xrow;
    u32 epoch;
    asm volatile("s_load_dword %0, %1, 0x0" : "=s"(epoch) : "s"(ws) : "memory");
    ld_x4(xrow, x, xrow_off);
    if (TA) { gb1[0] = 0; gb1[1] = 0; }
    else { ld_u16(gb1[0], b1, gb1_off); ld_u16(gb1[1], b1, gb1_off + 32u); }
    auto issue = [&](int s, u32 sb) {
      const int ks = (s + rot) & 7;
      const char* xs = xsrc + ks * 128;
      const char* wsb = wsrc + (size_t)((u32)ks * 64u * wstride);
      glds_m0<0>(m0x + sb, xs, vx0);
      glds<1024>(xs, vx1);
      glds_m0<0>(m0w + sb, wsb, vws[0]);
      glds<1024>(wsb, vws[1]);
      glds<2048>(wsb, vws[2]);
      if (!TA) glds_masked<3072>(wsb, vws[3], hmask);
    };
    issue(0, 0); issue(1, SLOT); issue(2, 2 * SLOT);
    if (TA && w < 2) glds_m0<0>(lb + L_WPHI + (u32)w * 1024u, wphi + g * 2048 + w * 1024, (u32)l * 16u);

#define MF(S, t, rt) acc[t][rt] = mfma32(cat(flo[S][t], fhi[S][t]), fx[S][rt], acc[t][rt])
#define MFS(S, t, rt) acc[t][rt] = mfma32(fx[S][rt], cat(flo[S][t], fhi[S][t]), acc[t][rt])
#define RW(N, t) do { if (RD) { dsrtr<(N) * 16384>(flo[N][t], R[t]); dsrtr<(N) * 16384 + 512>(fhi[N][t], R[t]); } } while (0)
#define RX(N) do { if (RD) { dsr128<0>(fx[N][0], (N) ? xa1 : xa0); dsr128<2048>(fx[N][1], (N) ? xa1 : xa0); } } while (0)
    auto waitS = [&](auto Sc) {
      constexpr int S = decltype(Sc)::value;
      if constexpr (N8) waitset<8>(fx[S][0], fx[S][1], flo[S], fhi[S]);
      else waitset<6>(fx[S][0], fx[S][1], flo[S], fhi[S]);
    };
    auto phase = [&](auto Sc, auto RDc, auto GLc, const char* xs, const char* wsb, u32 sb) {
      constexpr int S = decltype(Sc)::value, N = 1 - S;
      constexpr bool RD = decltype(RDc)::value != 0;
      constexpr int GL = decltype(GLc)::value;
      waitS(Sc);
      if (S == 1 && RD && !EXP_NOBAR) BAR();
      SCHED();
#define DM(i) do { if (EXP_NODMA) break; \
        if (GL == 1 && (i) == 0) glds_m0<0>(m0x + sb, xs, vx0); \
        if (GL == 1 && (i) == 1) glds<1024>(xs, vx1); \
        if (GL == 1 && (i) == 2) glds_m0<0>(m0w + sb, wsb, vws[0]); \
        if (GL == 2 && (i) == 0) glds_m0<1024>(m0w + sb, wsb, vws[1]); \
        if (GL == 2 && (i) == 1) glds<2048>(wsb, vws[2]); \
        if (GL == 2 && (i) == 2 && !TA) glds_masked<3072>(wsb, vws[3], hmask); } while (0)
      if constexpr (N8) {
        MF(S, 6, 0); RW(N, 6); SCHED();
        MF(S, 6, 1); RW(N, 7); SCHED();
        MF(S, 7, 0); DM(0); SCHED();
        MF(S, 7, 1); SCHED();
      }
      if constexpr (TA) {
        MFS(S, 4, 0); RX(N); SCHED();
        MFS(S, 4, 1); RW(N, 4); SCHED();
        MFS(S, 5, 0); if (!N8) DM(0); SCHED();
        MFS(S, 5, 1); RW(N, 5); SCHED();
      } else {
        MF(S, 4, 0); RX(N); SCHED();
        MF(S, 4, 1); RW(N, 4); SCHED();
        MF(S, 5, 0); if (!N8) DM(0); SCHED();
        MF(S, 5, 1); RW(N, 5); SCHED();
      }
      MF(S, 0, 0); RW(N, 0); SCHED();
      MF(S, 0, 1); RW(N, 1); SCHED();
      MF(S, 1, 0); DM(1); SCHED();
      MF(S, 1, 1); RW(N, 2); SCHED();
      MF(S, 2, 0); RW(N, 3); SCHED();
      MF(S, 2, 1); SCHED();
      MF(S, 3, 0); DM(2); SCHED();
      MF(S, 3, 1); SCHED();
#undef DM
    };
    auto advance = [&](u32 d) {
#pragma unroll
      for (int t = 0; t < (N8 ? 8 : 6); ++t) R[t] += d;
      xa0 += d; xa1 += d;
    };

    STAMP(1);
    if (TA) asm volatile("s_waitcnt vmcnt(%1)" : "+v"(xrow) : "n"(2 * NG) : "memory");
    else asm volatile("s_waitcnt vmcnt(%3)" : "+v"(xrow), "+v"(gb1[0]), "+v"(gb1[1]) : "n"(2 * NG) : "memory");
    asm volatile("" ::"v"(pfd));
    STAMPG(1);
    BAR();
    STAMPG(2);
    {
      constexpr bool RD = true;
      RX(0); RW(0, 4); RW(0, 5); RW(0, 0); RW(0, 1); RW(0, 2); RW(0, 3);
      if constexpr (N8) { RW(0, 6); RW(0, 7); }
    }
    waitS(IC<0>{});
    asm volatile("" : "+s"(epoch) :: "memory");
    tag = epoch + 1u;
    if (threadIdx.x == 0) *(volatile AS3 u32*)(L + L_OFL) = epoch;
    phase(IC<0>{}, IC<1>{}, IC<0>{}, nullptr, nullptr, 0u);
    {
      float v[8];
#pragma unroll
      for (int i = 0; i < 4; ++i) { v[2 * i] = bflo(xrow[i]); v[2 * i + 1] = bfhi(xrow[i]); }
      const float mean = wave_sum(((v[0] + v[1]) + (v[2] + v[3])) + ((v[4] + v[5]) + (v[6] + v[7]))) * (1.0f / 512.0f);
      float qq = 0.f;
#pragma unroll
      for (int i = 0; i < 8; ++i) { float d = v[i] - mean; qq = fmaf(d, d, qq); }
      const float rstd = 1.0f / sqrtf(wave_sum(qq) * (1.0f / 512.0f) + 1e-5f);
      if (l == 0) {
        u32x4 gr = {fbits(mean), fbits(rstd), tag, tag};
        st_sc1_x4(ws, WS_STAT + (u32)(128 * p + 8 * c + w) * 16u, gr);
        if (w == 7) st_sc1_x1(ws, WS_AFLAG + (u32)b * 4u, tag);
      }
    }
    auto colsum = [&](int s, u32 sb) {
      if (p < 8 && (p >> 1) == s && w < (TA ? 6 : 7)) {
        u32x2 c0l[2], c0h[2], c1l[2], c1h[2];
        const u32 a0 = (vbase ^ (u32)((2 * (w & 1)) * 32)) + (u32)(w >> 1) * 1024u + sb;
        const u32 a1 = a0 ^ 32u;
        dsrtr<0>(c0l[0], a0); dsrtr<512>(c0h[0], a0); dsrtr<16384>(c0l[1], a0); dsrtr<16384 + 512>(c0h[1], a0);
        dsrtr<0>(c1l[0], a1); dsrtr<512>(c1h[0], a1); dsrtr<16384>(c1l[1], a1); dsrtr<16384 + 512>(c1h[1], a1);
        asm volatile("s_waitcnt lgkmcnt(0)" : "+v"(c0l[0]), "+v"(c0h[0]), "+v"(c0l[1]), "+v"(c0h[1]),
                     "+v"(c1l[0]), "+v"(c1h[0]), "+v"(c1l[1]), "+v"(c1h[1]) :: "memory");
        f32x4 c0 = {0, 0, 0, 0}, c1 = {0, 0, 0, 0};
#pragma unroll
        for (int ks = 0; ks < 2; ++ks) {
          c0 = mfma32(ones, cat(c0l[ks], c0h[ks]), c0);
          c1 = mfma32(ones, cat(c1l[ks], c1h[ks]), c1);
        }
        if (l < 32) {
          u32x2 gr = {fbits(l < 16 ? c0[0] : c1[0]), tag};
          st_sc1_x2(ws, WS_CS + (u32)((c * 256 + 32 * w + l) * 8 + ((s + rot) & 7)) * 8u, gr);
        }
      }
    };
    colsum(0, 0u);
    u32 sb = 0;
#pragma unroll 1
    for (int s = 0; s < 5; ++s) {
      const u32 nsb = sb == 2 * SLOT ? 0u : sb + SLOT;
      advance(nsb - sb);
      const int ks = (s + 3 + rot) & 7;
      WAIT_V(NG);
      STAMPG(3 + s);
      phase(IC<1>{}, IC<1>{}, IC<1>{}, xsrc + ks * 128, wsrc + (size_t)((u32)ks * 64u * wstride), sb);
      phase(IC<0>{}, IC<1>{}, IC<2>{}, xsrc + ks * 128, wsrc + (size_t)((u32)ks * 64u * wstride), sb);
      colsum(s + 1, nsb);
      sb = nsb;
    }
    advance(0u - 2u * SLOT);
    WAIT_V(NG);
    STAMPG(8);
    ld_sc1_x4(gat[0], ws, gcs);
    ld_sc1_x4(gat[1], ws, gcs + 1024u);
    ld_sc1_x4(gat[2], ws, gst);
    if (PF_MODE == 1) prefetchW();
    if (PF_EARLY) {
      const u32 wi = (u32)(bi * 8 + w), ix = (wi & 31u) * 16u + (u32)(l & 15);
      pf_lds(lb + L_PF, w2, (wi >> 5) * 229376u + (ix >> 2) * 1024u + ((ix & 3u) * 2u + 1u) * 128u, 0xffffull);
    }
    phase(IC<1>{}, IC<1>{}, IC<0>{}, nullptr, nullptr, 0u);
    if (TA) issueB(0);
    phase(IC<0>{}, IC<1>{}, IC<0>{}, nullptr, nullptr, 0u);
    advance(SLOT);
    WAIT_V((TA ? 7 : 3) + PFN);
    STAMPG(9);
    phase(IC<1>{}, IC<1>{}, IC<0>{}, nullptr, nullptr, 0u);
    if (TA) issueB(1);
    phase(IC<0>{}, IC<1>{}, IC<0>{}, nullptr, nullptr, 0u);
    asm volatile("s_waitcnt vmcnt(%3)" : "+v"(gat[0]), "+v"(gat[1]), "+v"(gat[2]) : "n"((TA ? 8 : 0) + PFN) : "memory");
    {
      const bool used = w < (TA ? 6 : 7);
#pragma unroll 1
      for (int it = 0; it < SPIN_MAX; ++it) {
        const bool bad = (used && ((gat[0][1] != tag) | (gat[0][3] != tag) | (gat[1][1] != tag) | (gat[1][3] != tag))) | (gat[2][2] != tag);
        if (__builtin_amdgcn_ballot_w64(bad) == 0ull) break;
        ld_sc1_x4(gat[0], ws, gcs);
        ld_sc1_x4(gat[1], ws, gcs + 1024u);
        ld_sc1_x4(gat[2], ws, gst);
        asm volatile("s_waitcnt vmcnt(0)" : "+v"(gat[0]), "+v"(gat[1]), "+v"(gat[2]) :: "memory");
      }
#pragma unroll
      for (int i = 0; i < 2; ++i) {
        float sm = bitsf(gat[i][0]) + bitsf(gat[i][2]);
        sm += bitsf((u32)__builtin_amdgcn_update_dpp(0, (int)fbits(sm), 0xB1, 0xf, 0xf, false));
        sm += bitsf((u32)__builtin_amdgcn_update_dpp(0, (int)fbits(sm), 0x4E, 0xf, 0xf, false));
        if ((l & 3) == 0) {
          f32x2 o = {sm, bflo(gb1[i])};
          *(AS3 f32x2*)(L + L_CSB + (u32)(32 * w + 16 * i + (l >> 2)) * 8u) = o;
        }
      }
      if (l < 16) {
        *(AS3 float*)(L + L_MEAN + (u32)(16 * w + l) * 4u) = bitsf(gat[2][0]);
        *(AS3 float*)(L + L_RSTD + (u32)(16 * w + l) * 4u) = bitsf(gat[2][1]);
      }
    }
    waitS(IC<1>{});
    BAR();
    SCHED();
    if constexpr (N8) { MF(1, 6, 0); MF(1, 6, 1); MF(1, 7, 0); MF(1, 7, 1); SCHED(); }
    if constexpr (TA) {
      MFS(1, 4, 0); issueBk(2, 0); SCHED(); MFS(1, 4, 1); issueBk(2, 1); SCHED();
      MFS(1, 5, 0); issueBk(2, 2); SCHED(); MFS(1, 5, 1); issueBk(2, 3); SCHED();
    } else {
      MF(1, 4, 0); MF(1, 4, 1); MF(1, 5, 0); MF(1, 5, 1); SCHED();
    }
    MF(1, 2, 0); if (TA) issueBk(3, 0); SCHED(); MF(1, 2, 1); if (TA) issueBk(3, 1); SCHED();
    MF(1, 3, 0); if (TA) issueBk(3, 2); SCHED(); MF(1, 3, 1); if (TA) issueBk(3, 3); SCHED();
    MF(1, 0, 0); MF(1, 0, 1); MF(1, 1, 0); MF(1, 1, 1); SCHED();
#undef MF
#undef MFS
#undef RW
#undef RX
    STAMP(2);
    STAMPG(10);
  };
  if (typeA) g1(IC<1>{}, IC<0>{});
  else if (cg == 0) g1(IC<0>{}, IC<1>{});
  else g1(IC<0>{}, IC<0>{});
  STAMP(3);

  const float mean0 = *(AS3 float*)(L + L_MEAN + (u32)(32 * rg + r16) * 4u);
  const float mean1 = *(AS3 float*)(L + L_MEAN + (u32)(32 * rg + 16 + r16) * 4u);
  const float rstd0 = *(AS3 float*)(L + L_RSTD + (u32)(32 * rg + r16) * 4u);
  const float rstd1 = *(AS3 float*)(L + L_RSTD + (u32)(32 * rg + 16 + r16) * 4u);

  const u32 vb2 = (u32)(4 * gq + q4) * 128u + (u32)pp * 8u + (u32)((2 * gq + (q4 >> 1)) & 3) * 32u;
  u32x2 blo[2][4], bhi[2][4];
  u32 VB[4];
#pragma unroll
  for (int ct = 0; ct < 4; ++ct) VB[ct] = lb + (vb2 ^ (u32)(ct * 32)) + (u32)w * 4096u;
  auto rdB = [&](auto jc) {
    constexpr int j = decltype(jc)::value, blk = (j + 3) & 3;
#pragma unroll
    for (int ct = 0; ct < 4; ++ct) {
      const u32 a = VB[ct] + (blk >= 2 ? 65536u : 0u);
      dsrtr<(blk & 1) * 32768>(blo[j & 1][ct], a);
      dsrtr<(blk & 1) * 32768 + 2048>(bhi[j & 1][ct], a);
    }
  };
  const int dp = rb >> 1;

  const char* af = ws + WS_AF + (size_t)(4 * rb) * 73728u;
  const char* afh = af + (size_t)(7 * w) * 1024u;
  const char* afo = af + (size_t)(56 + 2 * w) * 1024u;
  const u32 vo = (u32)l * 16u;
  u32x4 a[9][4];
  auto loadA = [&](int j) {
    const char* s = j < 7 ? afh + j * 1024 : afo + (j - 7) * 1024;
#pragma unroll
    for (int rt = 0; rt < 4; ++rt) ld_sc1_x4(a[j][rt], s + rt * 73728, vo);
  };
  auto waitA01 = [&]() {
    asm volatile("s_waitcnt vmcnt(0)" : "+v"(a[0][0]), "+v"(a[0][1]), "+v"(a[0][2]), "+v"(a[0][3]), "+v"(a[1][0]), "+v"(a[1][1]), "+v"(a[1][2]), "+v"(a[1][3])
                 :: "memory");
  };
  u32x4 phq[2][2], zg;
  if (!typeA) {
    auto hpair = [&](auto Pc) {
      constexpr int P = decltype(Pc)::value;
      const int hl = cg * 4 + P;
      u32x4 o0, o1;
#pragma unroll
      for (int e = 0; e < 2; ++e) {
        const int t = 2 * P + e;
        const u32 ca = L_CSB + (u32)(hl * 32 + e * 16 + 4 * gq) * 8u;
        const f32x4 cb0 = lds128f(L, ca), cb1 = lds128f(L, ca + 16u);
        const float cs[4] = {cb0[0], cb0[2], cb1[0], cb1[2]}, bb[4] = {cb0[1], cb0[3], cb1[1], cb1[3]};
        float h0[4], h1[4];
#pragma unroll
        for (int r = 0; r < 4; ++r) {
          float y0 = fmaf(rstd0, fmaf(-mean0, cs[r], acc[t][0][r]), bb[r]);
          float y1 = fmaf(rstd1, fmaf(-mean1, cs[r], acc[t][1][r]), bb[r]);
          y0 = fmaxf(y0, 0.f); y1 = fmaxf(y1, 0.f);
          h0[r] = y0 * y0; h1[r] = y1 * y1;
        }
        o0[2 * e] = pk2(h0[0], h0[1]); o0[2 * e + 1] = pk2(h0[2], h0[3]);
        o1[2 * e] = pk2(h1[0], h1[1]); o1[2 * e + 1] = pk2(h1[2], h1[3]);
      }
      const u32 step = (u32)(7 * g + ((g & 1) ? (hl < 6 ? hl + 1 : 0) : hl));
      const u32 off = WS_AF + ((u32)(8 * p + 2 * rg) * 72u + step) * 1024u + (u32)l * 16u;
      st_sc1_x4(ws, off, o0);
      st_sc1_x4(ws, off + 72u * 1024u, o1);
    };
    hpair(IC<0>{}); hpair(IC<1>{}); hpair(IC<2>{});
    if (cg == 0) hpair(IC<3>{});
    WAIT_V(0);
    BAR();
    if (threadIdx.x == 0) st_sc1_x1(ws, WS_HFLAG + (u32)p * 128u + (u32)g * 4u, tag);
    STAMP(4);
    if (w == 0) poll_flags(ws, WS_HFLAG + (u32)dp * 128u, 8, l, tag);
    if (PF_MODE == 2 || PF_MODE == 4 || PF_MODE == 6) prefetchW();
    issueB(0); issueB(1); issueB(2); issueB(3);
    if (PF_MODE == 3) prefetchW();
    WAIT_V(8);
    rdB(IC<0>{}); rdB(IC<1>{});
    waitb(blo[0], bhi[0]); waitb(blo[1], bhi[1]);
    issueB(4); issueB(5);
    BAR();
    STAMP(5);
    loadA(0); loadA(1);
    waitA01();
    STAMP(7);
  } else {
    const u32x2 ident = {(4 * gq + 0 == r16 ? 0x3F80u : 0u) | (4 * gq + 1 == r16 ? 0x3F800000u : 0u),
                         (4 * gq + 2 == r16 ? 0x3F80u : 0u) | (4 * gq + 3 == r16 ? 0x3F800000u : 0u)};
    const f32x4 zero4 = {0, 0, 0, 0};
    const f32x4 vm0 = lds128f(L, L_MEAN + (u32)(32 * rg + 4 * gq) * 4u), vm1 = lds128f(L, L_MEAN + (u32)(32 * rg + 16 + 4 * gq) * 4u);
    const f32x4 vr0 = lds128f(L, L_RSTD + (u32)(32 * rg + 4 * gq) * 4u), vr1 = lds128f(L, L_RSTD + (u32)(32 * rg + 16 + 4 * gq) * 4u);
    u32x2 wp[2];
    auto logits = [&](int line, int pi, int rt) -> f32x4 {
      const int t = line * 2 + pi;
      const u32 ca = L_CSB + (u32)(line * 64 + (2 * cg + pi) * 16 + 4 * gq) * 8u;
      const f32x4 cb0 = lds128f(L, ca), cb1 = lds128f(L, ca + 16u);
      const float mn = rt ? mean1 : mean0, rs = rt ? rstd1 : rstd0;
      const float y0 = rs * fmaf(-mn, cb0[0], acc[t][rt][0]), y1 = rs * fmaf(-mn, cb0[2], acc[t][rt][1]);
      const float y2 = rs * fmaf(-mn, cb1[0], acc[t][rt][2]), y3 = rs * fmaf(-mn, cb1[2], acc[t][rt][3]);
      const u32x2 kb = {pk2(y0, y1), pk2(y2, y3)};
      return mfma16(wp[pi], kb, zero4);
    };
#pragma unroll
    for (int pi = 0; pi < 2; ++pi)
      wp[pi] = lds_tr(L, L_WPHI + (u32)((2 * cg + pi) * 512 + (4 * gq + q4) * 32 + pp * 8));
    f32x4 kvt[4], zt[4];
    if (PF_MODE >= 5 && rg == 3) prefetchK(0);
#pragma unroll
    for (int pi = 0; pi < 2; ++pi) {
      u32x4 tp, tn, vv;
#pragma unroll
      for (int rt = 0; rt < 2; ++rt) {
        const u32x4 ph = dual_softmax(logits(1, pi, rt));
        if (PF_MODE >= 5 && rg == 3) prefetchK(1 + 2 * pi + rt);
        const f32x4 a = mfma16(u32x2{ph[0], ph[1]}, ident, zero4);
        const f32x4 n = mfma16(u32x2{ph[2], ph[3]}, ident, zero4);
        tp[2 * rt] = pk2(a[0], a[1]); tp[2 * rt + 1] = pk2(a[2], a[3]);
        tn[2 * rt] = pk2(n[0], n[1]); tn[2 * rt + 1] = pk2(n[2], n[3]);
        const float cs = *(AS3 float*)(L + L_CSB + (u32)(128 + (2 * cg + pi) * 16 + r16) * 8u);
        const f32x4 vm = rt ? vm1 : vm0, vr = rt ? vr1 : vr0;
        const f32x4 av = acc[4 + pi][rt];
        vv[2 * rt] = pk2(vr[0] * fmaf(-vm[0], cs, av[0]), vr[1] * fmaf(-vm[1], cs, av[1]));
        vv[2 * rt + 1] = pk2(vr[2] * fmaf(-vm[2], cs, av[2]), vr[3] * fmaf(-vm[3], cs, av[3]));
      }
      kvt[2 * pi] = mfma32(tp, vv, zero4); kvt[2 * pi + 1] = mfma32(tn, vv, zero4);
      zt[2 * pi] = mfma32(tp, ones, zero4); zt[2 * pi + 1] = mfma32(tn, ones, zero4);
    }
    {
      const int sl = rg > 0 ? rg - 1 : 0;
#pragma unroll
      for (int pi = 0; pi < 2; ++pi) {
        if (rg != pi) {
#pragma unroll
          for (int sg = 0; sg < 2; ++sg)
            *(AS3 f32x4*)(L + L_SCR + (u32)((((cg * 2 + pi) * 2 + sg) * 3 + sl) * 1024 + l * 16)) = kvt[2 * pi + sg];
        }
        if (r16 == 0) {
#pragma unroll
          for (int sg = 0; sg < 2; ++sg)
            *(AS3 f32x4*)(L + L_ZR + (u32)(((((cg * 2 + pi) * 2 + sg) * 4 + rg) * 16 + 4 * gq) * 4)) = zt[2 * pi + sg];
        }
      }
    }
    WAIT_L0();
    BAR();
    if (rg < 2) {
      f32x4 sp = rg == 0 ? kvt[0] : kvt[2], sn = rg == 0 ? kvt[1] : kvt[3];
#pragma unroll
      for (int sl = 0; sl < 3; ++sl) {
        sp += lds128f(L, L_SCR + (u32)((((cg * 2 + rg) * 2 + 0) * 3 + sl) * 1024 + l * 16));
        sn += lds128f(L, L_SCR + (u32)((((cg * 2 + rg) * 2 + 1) * 3 + sl) * 1024 + l * 16));
      }
      u32x4 o = {pk2(sp[0], sp[1]), pk2(sp[2], sp[3]), pk2(sn[0], sn[1]), pk2(sn[2], sn[3])};
      st_sc1_x4(ws, WS_KVP + (u32)(((g * 16 + p) * 4 + 2 * cg + rg) * 1024 + l * 16), o);
    } else if (rg == 2) {
      const int pi = (l >> 3) & 1, sg = (l >> 2) & 1, qq = l & 3;
      f32x4 s = {0, 0, 0, 0};
#pragma unroll
      for (int r = 0; r < 4; ++r) s += lds128f(L, L_ZR + (u32)(((((cg * 2 + pi) * 2 + sg) * 4 + r) * 16 + 4 * qq) * 4));
      if (l < 16) st_sc1_x4(ws, WS_ZP + (u32)((((g * 4 + 2 * cg + pi) * 16 + p) * 32 + (l & 7) * 4) * 4), __builtin_bit_cast(u32x4, s));
    } else {
      WAIT_V(0);
      rdB(IC<0>{}); rdB(IC<1>{});
      waitb(blo[0], bhi[0]); waitb(blo[1], bhi[1]);
      issueB(4); issueB(5);
    }
#pragma unroll
    for (int rt = 0; rt < 2; ++rt) phq[0][rt] = dual_softmax(logits(0, 0, rt));
    if (rg != 3) WAIT_V(0);
    BAR();
    if (threadIdx.x == 0) st_sc1_x1(ws, WS_KVFLAG + (u32)g * 128u + (u32)p * 4u, tag);
    STAMP(4);
    if (rg != 3) {
      rdB(IC<0>{}); rdB(IC<1>{});
      waitb(blo[0], bhi[0]); waitb(blo[1], bhi[1]);
      issueB(4); issueB(5);
    }
#pragma unroll
    for (int rt = 0; rt < 2; ++rt) phq[1][rt] = dual_softmax(logits(0, 1, rt));
    if (w == 3)
      poll_o(ws, (l & 31) < 16 ? WS_KVFLAG + (u32)g * 128u + (u32)(l & 15) * 4u : WS_HFLAG + (u32)dp * 128u + (u32)(l & 7) * 4u, tag);
    BAR();
    STAMP(5);
    {
      const int hd = w & 3, hf = w >> 2;
#pragma unroll
      for (int i = 0; i < 8; ++i) ld_sc1_x4(gat[i], ws, WS_KVP + (u32)(((g * 16 + 8 * hf + i) * 4 + hd) * 1024 + l * 16));
      ld_sc1_x4(zg, ws, WS_ZP + (u32)(((g * 4 + hd) * 16 + 8 * hf) * 128 + l * 16));
      BAR();
      loadA(0); loadA(1);
      asm volatile("s_waitcnt vmcnt(8)" : "+v"(gat[0]), "+v"(gat[1]), "+v"(gat[2]), "+v"(gat[3]), "+v"(gat[4]), "+v"(gat[5]),
                   "+v"(gat[6]), "+v"(gat[7]), "+v"(zg) :: "memory");
    }
    STAMP(6);
    {
      const int hd = w & 3, hf = w >> 2;
      f32x4 s0 = {0, 0, 0, 0}, s1 = {0, 0, 0, 0};
#pragma unroll
      for (int i = 0; i < 8; ++i) {
        s0[0] += bflo(gat[i][0]); s0[1] += bfhi(gat[i][0]); s0[2] += bflo(gat[i][1]); s0[3] += bfhi(gat[i][1]);
        s1[0] += bflo(gat[i][2]); s1[1] += bfhi(gat[i][2]); s1[2] += bflo(gat[i][3]); s1[3] += bfhi(gat[i][3]);
      }
      *(AS3 f32x4*)(L + L_SCR + (u32)((hd * 2 + hf) * 2048 + l * 32)) = s0;
      *(AS3 f32x4*)(L + L_SCR + (u32)((hd * 2 + hf) * 2048 + l * 32 + 16)) = s1;
      f32x4 zs = __builtin_bit_cast(f32x4, zg);
#pragma unroll
      for (int r = 0; r < 4; ++r) {
        float v = zs[r];
        v += __shfl_xor(v, 8); v += __shfl_xor(v, 16); v += __shfl_xor(v, 32);
        zs[r] = v;
      }
      if (l < 8) *(AS3 f32x4*)(L + L_ZR + (u32)(((hd * 2 + hf) * 32 + l * 4) * 4)) = zs;
    }
    WAIT_L0();
    BAR();
    {
      u32x4 o0, o1;
#pragma unroll
      for (int pi = 0; pi < 2; ++pi) {
        const int hd = 2 * cg + pi;
        const u32 ga = L_SCR + (u32)(hd * 4096 + l * 32);
        const f32x4 kp = lds128f(L, ga) + lds128f(L, ga + 2048u), kn = lds128f(L, ga + 16u) + lds128f(L, ga + 2064u);
        const u32 za = L_ZR + (u32)((hd * 64 + 4 * gq) * 4);
        const f32x4 zp = lds128f(L, za) + lds128f(L, za + 128u), zn = lds128f(L, za + 64u) + lds128f(L, za + 192u);
        const u32x4 kf = {pk2(kp[0], kp[1]), pk2(kp[2], kp[3]), pk2(kn[0], kn[1]), pk2(kn[2], kn[3])};
        const u32x4 zf = {pk2(zp[0], zp[1]), pk2(zp[2], zp[3]), pk2(zn[0], zn[1]), pk2(zn[2], zn[3])};
#pragma unroll
        for (int rt = 0; rt < 2; ++rt) {
          const f32x4 num = mfma32(kf, phq[pi][rt], zero4), den = mfma32(zf, phq[pi][rt], zero4);
          const float rd = __builtin_amdgcn_rcpf(den[0] + 1e-6f);
          const u32 a = pk2(num[0] * rd, num[1] * rd), bq = pk2(num[2] * rd, num[3] * rd);
          if (rt == 0) { o0[2 * pi] = a; o0[2 * pi + 1] = bq; } else { o1[2 * pi] = a; o1[2 * pi + 1] = bq; }
        }
      }
      const u32 off = WS_AF + ((u32)(8 * p + 2 * rg) * 72u + (u32)(56 + 2 * g + cg)) * 1024u + (u32)l * 16u;
      waitA01();
      st_sc1_x4(ws, off, o0);
      st_sc1_x4(ws, off + 72u * 1024u, o1);
    }
    BAR();
    STAMP(7);
  }
  {
    u32x4 xres, b2v;
    const u32 fo = (u32)(64 * rb + (threadIdx.x >> 3)) * 1024u + (u32)(cb * 128 + (threadIdx.x & 7) * 16);
    f32x4 dacc[4][4];
#pragma unroll
    for (int i = 0; i < 4; ++i)
#pragma unroll
      for (int j = 0; j < 4; ++j) dacc[i][j] = f32x4{0, 0, 0, 0};
    auto waitA = [&](int j, auto n) { wait4<decltype(n)::value>(a[j][0], a[j][1], a[j][2], a[j][3]); };
    auto step = [&](int j) {
#pragma unroll
      for (int ct = 0; ct < 4; ++ct) {
        const u32x4 bf = cat(blo[j & 1][ct], bhi[j & 1][ct]);
#pragma unroll
        for (int rt = 0; rt < 4; ++rt) dacc[rt][ct] = mfma32(bf, a[j][rt], dacc[rt][ct]);
      }
      SCHED();
    };
    loadA(2); loadA(3);
    step(0); rdB(IC<2>{});
    step(1); waitb(blo[0], bhi[0]); rdB(IC<3>{});
    WAIT_V(8);
    if (typeA && l == 0) st_sc1_x1(ws, WS_OFLAG + (u32)((p * 8 + g) * 8 + w) * 4u, tag);
    STAMP(8);
    issueB(6); loadA(4);
    waitA(2, IC<12>{}); step(2); waitb(blo[1], bhi[1]); rdB(IC<4>{}); issueB(7); loadA(5);
    waitA(3, IC<16>{}); step(3); waitb(blo[0], bhi[0]); rdB(IC<5>{}); issueB(8); loadA(6);
    waitA(4, IC<16>{}); step(4); waitb(blo[1], bhi[1]); rdB(IC<6>{});
    waitA(5, IC<8>{}); step(5); waitb(blo[0], bhi[0]); rdB(IC<7>{});
    waitA(6, IC<0>{}); step(6); waitb(blo[1], bhi[1]); rdB(IC<8>{});
    STAMP(9);
    if (w == 0) {
      poll_o(ws, WS_OFLAG + (u32)((dp * 8 + ((l >> 2) & 7)) * 8 + 2 * (rb & 1) + (l & 1) + 4 * ((l >> 1) & 1)) * 4u, tag);
      *(volatile AS3 u32*)(L + L_OFL) = tag;
    } else {
#pragma unroll 1
      for (int it = 0; it < SPIN_MAX * 8; ++it) {
        if ((u32)__builtin_amdgcn_readfirstlane((int)*(volatile AS3 u32*)(L + L_OFL)) == tag) break;
        __builtin_amdgcn_s_sleep(2);
      }
    }
    STAMP(10);
    loadA(7); loadA(8);
    ld_x4(xres, x, fo);
    ld_x4(b2v, b2, (u32)(cb * 128 + (threadIdx.x & 7) * 16));
    STAMP(13);
    waitA(7, IC<6>{}); step(7);
    waitA(8, IC<2>{}); waitb(blo[0], bhi[0]); step(8);
    STAMP(14);
    WAIT_L0();
#pragma unroll
    for (int rt = 0; rt < 4; ++rt)
#pragma unroll
      for (int ct = 0; ct < 4; ++ct)
        *(AS3 f32x4*)(L + dslot(rt) + (u32)(r16 * 256 + (((ct * 4 + gq) ^ r16) * 16))) = dacc[rt][ct];
    WAIT_L0();
    BAR();
    STAMP(11);
    {
      const int row = threadIdx.x >> 3, cq = threadIdx.x & 7, rt = row >> 4, tk = row & 15;
      const u32 pb = (u32)((rt + 3) & 3) * 32768u + (u32)(tk * 256);
      f32x4 s0 = {0, 0, 0, 0}, s1 = {0, 0, 0, 0};
#pragma unroll
      for (int ww = 0; ww < 8; ++ww) {
        s0 += lds128f(L, pb + (u32)(ww * 4096) + (u32)(((2 * cq) ^ tk) * 16));
        s1 += lds128f(L, pb + (u32)(ww * 4096) + (u32)(((2 * cq + 1) ^ tk) * 16));
      }
      asm volatile("s_waitcnt vmcnt(0)" : "+v"(xres), "+v"(b2v) :: "memory");
      u32x4 o;
      o[0] = pk2(s0[0] + bflo(xres[0]) + bflo(b2v[0]), s0[1] + bfhi(xres[0]) + bfhi(b2v[0]));
      o[1] = pk2(s0[2] + bflo(xres[1]) + bflo(b2v[1]), s0[3] + bfhi(xres[1]) + bfhi(b2v[1]));
      o[2] = pk2(s1[0] + bflo(xres[2]) + bflo(b2v[2]), s1[1] + bfhi(xres[2]) + bfhi(b2v[2]));
      o[3] = pk2(s1[2] + bflo(xres[3]) + bflo(b2v[3]), s1[3] + bfhi(xres[3]) + bfhi(b2v[3]));
      *(u32x4*)(out + fo) = o;
    }
    if (b == 255 && w == 7) {
#pragma unroll 1
      for (int i = 0; i < 4; ++i) poll_flags(ws, WS_AFLAG + (u32)i * 256u, 64, l, tag);
      if (l == 0) st_sc1_x1(ws, WS_EPOCH, tag);
    }
    STAMP(12);
    STAMP_FLUSH;
  }
}

extern "C" void kernel_launch(void* const* d_in, const int* in_sizes, int n_in, void* d_out, int out_size, void* d_ws,
                              size_t ws_size, hipStream_t stream) {
  (void)in_sizes; (void)n_in; (void)out_size; (void)ws_size;
  hipFuncSetAttribute((const void*)fused_kernel, hipFuncAttributeMaxDynamicSharedMemorySize, LDS_BYTES);
  fused_kernel<<<256, 512, LDS_BYTES, stream>>>((const char*)d_in[0], (const char*)d_in[1], (const char*)d_in[2],
                                                (const char*)d_in[3], (const char*)d_in[5], (const char*)d_in[6],
                                                (const char*)d_in[7], (const char*)d_in[8], (char*)d_out, (char*)d_ws);
}
```
